```python
import torch

def _softmax(x, axis=-1):
    return torch.softmax(x, dim=axis)
def _gelu(x):
    return torch.nn.functional.gelu(x, approximate="tanh")
def _silu(x):
    return torch.nn.functional.silu(x)
def _sigmoid(x):
    return torch.sigmoid(x)
def _layernorm(x, axis=-1, eps=1e-5):
    m = x.mean(dim=axis, keepdim=True)
    v = x.var(dim=axis, keepdim=True, unbiased=False)
    return (x - m) / torch.sqrt(v + eps)
def _scatter_add(tgt, idx, src, axis=0):
    return tgt.clone().index_add_(axis, idx, src)
def _segment_sum(src, seg, n):
    out = torch.zeros((n,) + src.shape[1:], dtype=src.dtype)
    return out.index_add_(0, seg, src)
def _pad(x, pw):
    flat = tuple(v for pair in reversed(pw) for v in pair)
    return torch.nn.functional.pad(x, flat)

kernel_name = 'parallel_sum__sliding_block_cached__glu_gated_bilinear__decode'

def setup_inputs():

    _w = (lambda a: (lambda t: t.to(torch.bfloat16) if t.is_floating_point() else t)(torch.as_tensor(a)))
    def _A_setup():
        B, Sq, D = 4, 1, 1024
        Hq, Hkv, Sk, Dh = 32, 4, 8192, 128
        assert Hq % Hkv == 0, (Hq, Hkv)
        return {
            "x": _w((lambda *s: torch.randn(*s, dtype=torch.bfloat16) * 0.05)(B, Sq, D)),
            "k_cache": _w((lambda *s: torch.randn(*s, dtype=torch.bfloat16) * 1.6)(B, Hkv, Sk, Dh)),
            "v_cache": _w((lambda *s: torch.randn(*s, dtype=torch.bfloat16) * 0.8)(B, Hkv, Sk, Dh)),
            "wq": _w((lambda *s: torch.randn(*s, dtype=torch.bfloat16) * 1.6)(D, Hq * Dh) * (D ** -0.5)),
            "wo": _w((lambda *s: torch.randn(*s, dtype=torch.bfloat16) * 0.8)(Hq * Dh, D) * ((Hq * Dh) ** -0.5)),
        }
    _A_in = _A_setup()

    def _B_setup():
        B, S, D, F = 4, 1, 1024, 3584
        return {
            "x": _w((lambda *s: torch.randn(*s, dtype=torch.bfloat16) * 0.05)(B, S, D)),
            "w_gate": _w((lambda *s: torch.randn(*s, dtype=torch.bfloat16) * 0.4)(D, F) * (D ** -0.5)),
            "w_up": _w((lambda *s: torch.randn(*s, dtype=torch.bfloat16) * 0.4)(D, F) * (D ** -0.5)),
            "w_down": _w((lambda *s: torch.randn(*s, dtype=torch.bfloat16) * 0.4)(F, D) * (F ** -0.5)),
        }
    _B_in = _B_setup()

    return {
        "A_x": _A_in["x"],
        "A_k_cache": _A_in["k_cache"],
        "A_v_cache": _A_in["v_cache"],
        "A_wq": _A_in["wq"],
        "A_wo": _A_in["wo"],
        "B_x": _B_in["x"],
        "B_w_gate": _B_in["w_gate"],
        "B_w_up": _B_in["w_up"],
        "B_w_down": _B_in["w_down"],
    }

def reference(A_x, A_k_cache, A_v_cache, A_wq, A_wo, B_x, B_w_gate, B_w_up, B_w_down):
    def _A_ref(x, k_cache, v_cache, wq, wo):
        B, Sq, D = x.shape
        Hq, Dh = 32, 128
        Hkv, Sk = k_cache.shape[1], k_cache.shape[2]
        W = min(4096, max(256, Sk // 4))
        rep = Hq // Hkv
        q = (lambda x, *d: x.permute(*d))((x @ wq).reshape(B, Sq, Hq, Dh), 0, 2, 1, 3)
        k = (lambda x, n, axis: torch.repeat_interleave(x, n, dim=axis))(k_cache[:, :, Sk - W:, :], rep, axis=1)
        v = (lambda x, n, axis: torch.repeat_interleave(x, n, dim=axis))(v_cache[:, :, Sk - W:, :], rep, axis=1)
        scores = torch.einsum("bhqd,bhkd->bhqk", q, k) * (Dh ** -0.5)
        p = _softmax(scores, axis=-1)
        o = torch.einsum("bhqk,bhkd->bhqd", p, v)
        return (lambda x, *d: x.permute(*d))(o, 0, 2, 1, 3).reshape(B, Sq, Hq * Dh) @ wo

    def _B_ref(x, w_gate, w_up, w_down):
        g = torch.einsum("bsd,df->bsf", x, w_gate)
        u = torch.einsum("bsd,df->bsf", x, w_up)
        h = g * u
        return torch.einsum("bsf,fd->bsd", h, w_down)

    x = A_x
    xn = _layernorm(x, axis=-1)
    return x + _A_ref(xn, A_k_cache, A_v_cache, A_wq, A_wo) + _B_ref(xn, B_w_gate, B_w_up, B_w_down)
```

```cpp
#include <hip/hip_runtime.h>
#include <hip/hip_bf16.h>
#include <stdint.h>

typedef unsigned short u16;
__device__ __forceinline__ float bf2f(u16 v) { return __uint_as_float(((unsigned)v) << 16); }
__device__ __forceinline__ u16 f2bf(float f) { __hip_bfloat16 h = __float2bfloat16(f); return *(u16*)&h; }
__device__ __forceinline__ float rbf(float f) { return bf2f(f2bf(f)); }

constexpr int B = 4, D = 1024, HQ = 32, HKV = 4, SK = 8192, DH = 128, W = 2048, F = 3584;

__global__ void k_ln(const u16* x, float* xn) {
  int b = blockIdx.x; int t = threadIdx.x;
  __shared__ float red[256];
  float v[4]; float s = 0;
  for (int i = 0; i < 4; i++) { v[i] = bf2f(x[b * D + t * 4 + i]); s += v[i]; }
  red[t] = s; __syncthreads();
  for (int o = 128; o > 0; o >>= 1) { if (t < o) red[t] += red[t + o]; __syncthreads(); }
  float m = red[0] / D; __syncthreads();
  float q = 0; for (int i = 0; i < 4; i++) { float d = v[i] - m; q += d * d; }
  red[t] = q; __syncthreads();
  for (int o = 128; o > 0; o >>= 1) { if (t < o) red[t] += red[t + o]; __syncthreads(); }
  float var = red[0] / D;
  float mb = rbf(m), vb = rbf(var);
  float den = rbf(sqrtf(rbf(vb + 1e-5f)));
  for (int i = 0; i < 4; i++) xn[b * D + t * 4 + i] = rbf(rbf(v[i] - mb) / den);
}
__global__ void k_gemv(const float* in, const u16* w, float* out, int K, int N) {
  int n = blockIdx.x * 64 + (threadIdx.x & 63); int ks = threadIdx.x >> 6;
  __shared__ float red[4][4][64];
  float a[4] = {0, 0, 0, 0};
  for (int k = ks; k < K; k += 4) { float wv = bf2f(w[(size_t)k * N + n]); for (int b = 0; b < 4; b++) a[b] += in[b * K + k] * wv; }
  for (int b = 0; b < 4; b++) red[ks][b][threadIdx.x & 63] = a[b];
  __syncthreads();
  if (ks == 0) for (int b = 0; b < 4; b++) { int l = threadIdx.x & 63; out[b * N + n] = rbf(red[0][b][l] + red[1][b][l] + red[2][b][l] + red[3][b][l]); }
}
__global__ void k_mul(const float* g, const float* u, float* h, int n) { int i = blockIdx.x * 256 + threadIdx.x; if (i < n) h[i] = rbf(g[i] * u[i]); }
__global__ void k_attn(const float* q, const u16* kc, const u16* vc, float* o) {
  int h = blockIdx.x, b = blockIdx.y, t = threadIdx.x; int g = h / 8;
  __shared__ float s[W]; __shared__ float red[256]; __shared__ float qs[DH];
  if (t < DH) qs[t] = q[b * 4096 + h * DH + t];
  __syncthreads();
  const u16* kb = kc + ((size_t)(b * HKV + g) * SK + (SK - W)) * DH;
  const u16* vb = vc + ((size_t)(b * HKV + g) * SK + (SK - W)) * DH;
  float mx = -INFINITY;
  for (int j = t; j < W; j += 256) { float a = 0; for (int d = 0; d < DH; d++) a += qs[d] * bf2f(kb[(size_t)j * DH + d]); a = rbf(rbf(a) * 0.08838834764831845f); s[j] = a; mx = fmaxf(mx, a); }
  red[t] = mx; __syncthreads();
  for (int o2 = 128; o2 > 0; o2 >>= 1) { if (t < o2) red[t] = fmaxf(red[t], red[t + o2]); __syncthreads(); }
  mx = red[0]; __syncthreads();
  float sm = 0; for (int j = t; j < W; j += 256) { float e = __expf(s[j] - mx); s[j] = e; sm += e; }
  red[t] = sm; __syncthreads();
  for (int o2 = 128; o2 > 0; o2 >>= 1) { if (t < o2) red[t] += red[t + o2]; __syncthreads(); }
  sm = red[0];
  if (t < DH) { float a = 0; for (int j = 0; j < W; j++) a += rbf(s[j] / sm) * bf2f(vb[(size_t)j * DH + t]); o[b * 4096 + h * DH + t] = rbf(a); }
}
__global__ void k_fin(const u16* x, const float* a, const float* f, u16* out) { int i = blockIdx.x * 256 + threadIdx.x; out[i] = f2bf(rbf(bf2f(x[i]) + a[i]) + f[i]); }

extern "C" void kernel_launch(void* const* d_in, const int* in_sizes, int n_in, void* d_out, int out_size, void* d_ws, size_t ws_size, hipStream_t stream) {
  const u16* x = (const u16*)d_in[0]; const u16* kc = (const u16*)d_in[1]; const u16* vc = (const u16*)d_in[2];
  const u16* wq = (const u16*)d_in[3]; const u16* wo = (const u16*)d_in[4];
  const u16* wg = (const u16*)d_in[6]; const u16* wu = (const u16*)d_in[7]; const u16* wd = (const u16*)d_in[8];
  float* ws = (float*)d_ws;
  float* xn = ws; float* q = xn + 4096; float* g = q + 16384; float* u = g + 4 * F; float* h = u + 4 * F; float* o = h + 4 * F; float* ao = o + 16384; float* fo = ao + 4096;
  k_ln<<<4, 256, 0, stream>>>(x, xn);
  k_gemv<<<4096 / 64, 256, 0, stream>>>(xn, wq, q, 1024, 4096);
  k_gemv<<<F / 64, 256, 0, stream>>>(xn, wg, g, 1024, F);
  k_gemv<<<F / 64, 256, 0, stream>>>(xn, wu, u, 1024, F);
  k_mul<<<(4 * F + 255) / 256, 256, 0, stream>>>(g, u, h, 4 * F);
  k_attn<<<dim3(32, 4), 256, 0, stream>>>(q, kc, vc, o);
  k_gemv<<<1024 / 64, 256, 0, stream>>>(o, wo, ao, 4096, 1024);
  k_gemv<<<1024 / 64, 256, 0, stream>>>(h, wd, fo, F, 1024);
  k_fin<<<16, 256, 0, stream>>>(x, ao, fo, (u16*)d_out);
}
```

```cpp
#include <hip/hip_runtime.h>
#include <stdint.h>

#define USE_NT 1
#define STAMP 0
#define NOPOLL 0
#define W2THR 4

#define GAS __attribute__((address_space(1)))
#define LAS __attribute__((address_space(3)))
typedef unsigned v4u __attribute__((ext_vector_type(4)));
typedef unsigned v2u __attribute__((ext_vector_type(2)));
typedef float f32x4 __attribute__((ext_vector_type(4)));
typedef __bf16 bf16x8 __attribute__((ext_vector_type(8)));
typedef unsigned short u16;

constexpr int DM = 1024, NQ = 4096, SK = 8192, DH = 128, WIN = 2048, FF = 3584;
constexpr int NWG = 256, NTHR = 768;

constexpr int WS_EPOCH = 0;
constexpr int WS_QP = 4096;
constexpr int WS_GU = WS_QP + 524288;
constexpr int WS_OP = WS_GU + 917504;
constexpr int WS_LS = WS_OP + 1048576;
constexpr int WS_OUT = WS_LS + 16384;
constexpr int WS_END = WS_OUT + 2097152;
constexpr int WS_STAMP = 8 << 20;

constexpr int L_FLAG = 0;
constexpr int L_TAG = 128;
constexpr int L_XN = 256;
constexpr int L_RED = L_XN + 8192;
constexpr int L_KST = L_RED + 24576;
constexpr int L_QB = L_KST + 32768;
constexpr int L_AO = L_QB + 2304;
constexpr int L_ML = L_AO + 4 * 8 * 528;
constexpr int L_OB = L_ML + 256;
constexpr int L_HB = L_OB + 256;
constexpr int L_END = L_HB + 256;
static_assert(L_END <= 160 * 1024 && L_END > 80 * 1024, "lds");
enum { F_XN = 0, F_Q, F_GU, F_QB, F_AT, F_O, F_H, F_ISS };

constexpr int AUX_SC1 = 16;
constexpr unsigned GSPIN = NOPOLL ? 1u : (1u << 14), LSPIN = 1u << 18;

__device__ __forceinline__ v4u ldw(const GAS void* p) {
  return __builtin_nontemporal_load((const GAS v4u*)p);
}
__device__ __forceinline__ float bflo(unsigned v) { return __uint_as_float(v << 16); }
__device__ __forceinline__ float bfhi(unsigned v) { return __uint_as_float(v & 0xffff0000u); }
__device__ __forceinline__ unsigned pk2(float lo, float hi) {
  typedef __bf16 bf2 __attribute__((ext_vector_type(2)));
  bf2 r; r.x = (__bf16)lo; r.y = (__bf16)hi;
  return __builtin_bit_cast(unsigned, r);
}
__device__ __forceinline__ float rbf(float f) { return (float)(__bf16)f; }
template <int CTRL> __device__ __forceinline__ float dpp(float x) {
  return __builtin_bit_cast(float, __builtin_amdgcn_mov_dpp(__builtin_bit_cast(int, x), CTRL, 0xf, 0xf, true));
}
constexpr int XOR1 = 0xB1, XOR2 = 0x4E, HMIR = 0x141, ROR8 = 0x128, ROR4 = 0x124;
__device__ __forceinline__ float xrow_sum(float x) {
  auto s = __builtin_amdgcn_permlane16_swap(__float_as_uint(x), __float_as_uint(x), false, false);
  x = __uint_as_float(s[0]) + __uint_as_float(s[1]);
  auto t = __builtin_amdgcn_permlane32_swap(__float_as_uint(x), __float_as_uint(x), false, false);
  return __uint_as_float(t[0]) + __uint_as_float(t[1]);
}
__device__ __forceinline__ float xrow_max(float x) {
  auto s = __builtin_amdgcn_permlane16_swap(__float_as_uint(x), __float_as_uint(x), false, false);
  x = fmaxf(__uint_as_float(s[0]), __uint_as_float(s[1]));
  auto t = __builtin_amdgcn_permlane32_swap(__float_as_uint(x), __float_as_uint(x), false, false);
  return fmaxf(__uint_as_float(t[0]), __uint_as_float(t[1]));
}
__device__ __forceinline__ float wave_sum(float x) {
  x += dpp<XOR1>(x); x += dpp<XOR2>(x); x += dpp<HMIR>(x); x += dpp<ROR8>(x);
  return xrow_sum(x);
}
__device__ __forceinline__ f32x4 mfma(v4u a, v4u b, f32x4 c) {
  return __builtin_amdgcn_mfma_f32_16x16x32_bf16(__builtin_bit_cast(bf16x8, a), __builtin_bit_cast(bf16x8, b), c, 0, 0, 0);
}
__device__ __forceinline__ void tpair(const v4u& r0, const v4u& r1, v4u (&F)[8], int m) {
#pragma unroll
  for (int d = 0; d < 4; d++) {
    F[2 * d][m] = __builtin_amdgcn_perm(r1[d], r0[d], 0x05040100u);
    F[2 * d + 1][m] = __builtin_amdgcn_perm(r1[d], r0[d], 0x07060302u);
  }
}
__device__ __forceinline__ void transpose8(const v4u (&R)[8], v4u (&F)[8]) {
#pragma unroll
  for (int m = 0; m < 4; m++) tpair(R[2 * m], R[2 * m + 1], F, m);
}
#define SB() __builtin_amdgcn_sched_barrier(0)
#define PIN8(F) asm volatile("" : "+v"(F[0]), "+v"(F[1]), "+v"(F[2]), "+v"(F[3]), "+v"(F[4]), "+v"(F[5]), "+v"(F[6]), "+v"(F[7]))
#define OPAQUE(x) asm volatile("" : "+v"(x) :: "memory")
__device__ __forceinline__ void lds_signal(LAS unsigned char* smem, int f, int w, int lane) {
  asm volatile("s_waitcnt lgkmcnt(0)" ::: "memory");
  if (lane == 0) *(volatile LAS unsigned*)(smem + L_FLAG + f * 16 + w * 4) = 1u;
}
__device__ __forceinline__ void lds_wait4(LAS unsigned char* smem, int f) {
#pragma unroll 1
  for (unsigned i = 0; i < LSPIN; i++) {
    const v4u v = *(volatile LAS v4u*)(smem + L_FLAG + f * 16);
    if (__builtin_amdgcn_readfirstlane(v[0] & v[1] & v[2] & v[3])) break;
  }
  asm volatile("" ::: "memory");
}
__device__ __forceinline__ unsigned get_tag(LAS unsigned char* smem) {
  v2u v;
#pragma unroll 1
  for (unsigned i = 0; i < LSPIN; i++) {
    v = *(volatile LAS v2u*)(smem + L_TAG);
    if (__builtin_amdgcn_readfirstlane(v[1])) break;
  }
  return __builtin_amdgcn_readfirstlane(v[0]);
}
__device__ __forceinline__ v4u ld_sc1(__amdgpu_buffer_rsrc_t rs, int off) {
  return __builtin_amdgcn_raw_buffer_load_b128(rs, off, 0, AUX_SC1);
}
__device__ __forceinline__ void st_sc1(__amdgpu_buffer_rsrc_t rs, int off, v4u v) { __builtin_amdgcn_raw_buffer_store_b128(v, rs, off, 0, AUX_SC1); }

#define TS(i)
#define TS_DECL
#define TS_OUT()

__global__ void __launch_bounds__(NTHR, 3)
fused_decode(const u16* __restrict__ x_, const u16* __restrict__ kc_, const u16* __restrict__ vc_, const u16* __restrict__ wq_,
             const u16* __restrict__ wo_, const u16* __restrict__ wg_, const u16* __restrict__ wu_, const u16* __restrict__ wd_,
             u16* __restrict__ out_, unsigned char* ws_) {
  __shared__ __attribute__((aligned(16))) unsigned char smem_[L_END];
  LAS unsigned char* smem = (LAS unsigned char*)smem_;
  GAS unsigned char* ws = (GAS unsigned char*)ws_;
  const int lane = threadIdx.x & 63;
  const int wave = __builtin_amdgcn_readfirstlane(threadIdx.x >> 6);
  const int wg = blockIdx.x, xc = wg & 7, y = wg >> 3;
  const int c16 = lane & 15, kq = lane >> 4;
  const __amdgpu_buffer_rsrc_t rs = __builtin_amdgcn_make_buffer_rsrc((void*)ws_, 0, WS_END, 0x00020000);
  const int half = (y >> 2) & 1, z = (y & 3) | ((y >> 3) << 2);
  const int h2 = xc * 4 + (z >> 2), u2 = z & 3;
  const bool has_wd = (z != 11) && (z != 15);
  const int v2 = xc * 14 + (z > 11 ? z - 1 : z);
  const f32x4 z4 = {0.f, 0.f, 0.f, 0.f};
  TS_DECL;
  TS(0);

  if (wave < 4) {
    const int w = wave;
    __builtin_amdgcn_s_setprio(3);
    const int ks = xc, hq = y;
    const bool has_f1 = y >= 4;
    const int fb = y >= 30 ? y - 30 : y - 2;
    const GAS unsigned char* xp = (const GAS unsigned char*)x_ + w * 2048 + lane * 16;
    v4u x0 = *(const GAS v4u*)xp, x1 = *(const GAS v4u*)(xp + 1024);
    const int krow = 128 * ks + 32 * w + 8 * kq;
    v4u R[8], G[8], U[8];
    {
      const GAS unsigned char* p = (const GAS unsigned char*)wq_ + (size_t)krow * (NQ * 2) + hq * 256 + c16 * 16;
#pragma unroll
      for (int i = 0; i < 8; i++) R[i] = ldw(p + (size_t)i * (NQ * 2));
    }
    SB();
    __builtin_amdgcn_s_barrier();
    SB();
    const __amdgpu_buffer_rsrc_t rg = __builtin_amdgcn_make_buffer_rsrc((void*)wg_, 0, has_f1 ? DM * FF * 2 : 0, 0x00020000);
    const __amdgpu_buffer_rsrc_t ru = __builtin_amdgcn_make_buffer_rsrc((void*)wu_, 0, has_f1 ? DM * FF * 2 : 0, 0x00020000);
    const int fo = krow * (FF * 2) + fb * 256 + c16 * 16;
    {
      float v[16];
#pragma unroll
      for (int i = 0; i < 4; i++) { v[2 * i] = bflo(x0[i]); v[2 * i + 1] = bfhi(x0[i]); v[8 + 2 * i] = bflo(x1[i]); v[9 + 2 * i] = bfhi(x1[i]); }
      float s = 0.f;
#pragma unroll
      for (int i = 0; i < 16; i++) s += v[i];
      const float mean = wave_sum(s) * (1.0f / DM);
      float q = 0.f;
#pragma unroll
      for (int i = 0; i < 16; i++) { const float d = v[i] - mean; q += d * d; }
      const float var = wave_sum(q) * (1.0f / DM);
      const float mb = rbf(mean);
      const float den = rbf(__builtin_sqrtf(rbf(rbf(var) + 1e-5f)));
      const float rinv = 1.0f / den;
      v4u o0, o1;
#pragma unroll
      for (int i = 0; i < 4; i++) {
        o0[i] = pk2(rbf(v[2 * i] - mb) * rinv, rbf(v[2 * i + 1] - mb) * rinv);
        o1[i] = pk2(rbf(v[8 + 2 * i] - mb) * rinv, rbf(v[9 + 2 * i] - mb) * rinv);
      }
      *(LAS v4u*)(smem + L_XN + w * 2048 + lane * 16) = o0;
      *(LAS v4u*)(smem + L_XN + w * 2048 + 1024 + lane * 16) = o1;
    }
    lds_signal(smem, F_XN, w, lane);
    SB();
#pragma unroll
    for (int i = 0; i < 8; i++) G[i] = __builtin_amdgcn_raw_buffer_load_b128(rg, fo + i * (FF * 2), 0, USE_NT ? 2 : 0);
    SB();
    TS(1);
    lds_wait4(smem, F_XN);
    const v4u xa = *(const LAS v4u*)(smem + L_XN + (c16 >> 2) * 2048 + krow * 2);
    auto tile = [&](const v4u (&W)[8], int mat) {
      v4u F[8];
      transpose8(W, F);
      float d[8];
#pragma unroll
      for (int j = 0; j < 8; j++) d[j] = mfma(xa, F[j], z4)[0];
      LAS f32x4* rp = (LAS f32x4*)(smem + L_RED + w * 6144 + mat * 2048 + kq * 512 + c16 * 32);
      rp[0] = f32x4{d[0], d[1], d[2], d[3]};
      rp[1] = f32x4{d[4], d[5], d[6], d[7]};
    };
    tile(R, 0);
    lds_signal(smem, F_Q, w, lane);
    TS(2);
    const unsigned tag = get_tag(smem);
    lds_wait4(smem, F_Q);
    if (lane < 32) {
      f32x4 a = z4;
#pragma unroll
      for (int ww = 0; ww < 4; ww++) a += *(const LAS f32x4*)(smem + L_RED + ww * 6144 + w * 512 + lane * 16);
      const v4u o = {pk2(a[0], a[1]), tag, pk2(a[2], a[3]), tag};
      st_sc1(rs, WS_QP + ((w * 4 + (hq >> 3)) * 8 + ks) * 4096 + (hq & 7) * 512 + lane * 16, o);
    }
    SB();
    TS(3);
#pragma unroll
    for (int i = 0; i < 8; i++) U[i] = __builtin_amdgcn_raw_buffer_load_b128(ru, fo + i * (FF * 2), 0, USE_NT ? 2 : 0);
    SB();
    if (has_f1) {
      tile(G, 1);
      SB();
      tile(U, 2);
      lds_signal(smem, F_GU, w, lane);
      TS(4);
      lds_wait4(smem, F_GU);
      const int mat = lane >> 5, ch = lane & 31;
      f32x4 a = z4;
#pragma unroll
      for (int ww = 0; ww < 4; ww++) a += *(const LAS f32x4*)(smem + L_RED + ww * 6144 + 2048 + mat * 2048 + w * 512 + ch * 16);
      const v4u o = {pk2(a[0], a[1]), tag, pk2(a[2], a[3]), tag};
      st_sc1(rs, WS_GU + (4 * fb + (ch >> 3)) * 8192 + w * 2048 + mat * 1024 + ks * 128 + (ch & 7) * 16, o);
    }
    TS(5);
    if (has_wd) {
      v4u g, u;
      int off = WS_GU + v2 * 8192 + w * 2048 + lane * 16;
#pragma unroll 1
      for (unsigned it = 0; it < GSPIN; it++) {
        __builtin_amdgcn_s_sleep(8);
        OPAQUE(off);
        g = ld_sc1(rs, off); u = ld_sc1(rs, off + 1024);
        const bool ok = (g[1] == tag) & (g[3] == tag) & (u[1] == tag) & (u[3] == tag);
        if (__all(ok)) break;
      }
      float gv[4] = {bflo(g[0]), bfhi(g[0]), bflo(g[2]), bfhi(g[2])}, uv[4] = {bflo(u[0]), bfhi(u[0]), bflo(u[2]), bfhi(u[2])};
      float hv[4];
#pragma unroll
      for (int r = 0; r < 4; r++) {
        float a = gv[r] + dpp<ROR8>(gv[r]); a = xrow_sum(a);
        float b = uv[r] + dpp<ROR8>(uv[r]); b = xrow_sum(b);
        hv[r] = rbf(a) * rbf(b);
      }
      if (lane < 8) *(LAS v2u*)(smem + L_HB + w * 64 + lane * 8) = v2u{pk2(hv[0], hv[1]), pk2(hv[2], hv[3])};
      lds_signal(smem, F_H, w, lane);
    }
    TS(6);
  } else if (wave < 8) {
    const int w = wave - 4;
    __builtin_amdgcn_s_barrier();
    __builtin_amdgcn_s_setprio(2);
    const int pair = xc * 2 + (y >> 4), sp = y & 15;
    const int b = pair >> 2, g = pair & 3;
    const size_t kv0 = ((size_t)pair * SK + (SK - WIN) + 128 * sp + 32 * w) * (DH * 2);
    v4u KR[8], VR[8], Q[8];
    int qoff = WS_QP + pair * 32768 + w * 1024 + lane * 16;
    lds_wait4(smem, F_Q);
#pragma unroll
    for (int i = 0; i < 8; i++) KR[i] = ldw((const GAS unsigned char*)kc_ + kv0 + i * 1024 + lane * 16);
    SB();
#pragma unroll
    for (int i = 0; i < 8; i++) Q[i] = ld_sc1(rs, qoff + i * 4096);
    SB();
#pragma unroll
    for (int i = 0; i < 8; i++) VR[i] = ldw((const GAS unsigned char*)vc_ + kv0 + (16 * (i >> 2) + 4 * kq + (i & 3)) * 256 + c16 * 16);
    SB();
    __builtin_amdgcn_s_setprio(3);
    lds_signal(smem, F_ISS, w, lane);
    TS(1);
    LAS unsigned char* kst = smem + L_KST + w * 8192;
#pragma unroll
    for (int i = 0; i < 8; i++) { const int key = 4 * i + kq; *(LAS v4u*)(kst + key * 256 + ((c16 ^ (key & 15)) * 16)) = KR[i]; }
    SB();
    const unsigned tag = get_tag(smem);
    {
      bool ok = true;
#pragma unroll
      for (int i = 0; i < 8; i++) ok &= (Q[i][1] == tag) & (Q[i][3] == tag);
      if (!__all(ok)) {
#pragma unroll 1
        for (unsigned it = 0; it < GSPIN; it++) {
          OPAQUE(qoff);
#pragma unroll
          for (int i = 0; i < 8; i++) Q[i] = ld_sc1(rs, qoff + i * 4096);
          ok = true;
#pragma unroll
          for (int i = 0; i < 8; i++) ok &= (Q[i][1] == tag) & (Q[i][3] == tag);
          if (__all(ok)) break;
        }
      }
      float a0 = 0.f, a1 = 0.f, a2 = 0.f, a3 = 0.f;
#pragma unroll
      for (int i = 0; i < 8; i++) { a0 += bflo(Q[i][0]); a1 += bfhi(Q[i][0]); a2 += bflo(Q[i][2]); a3 += bfhi(Q[i][2]); }
      *(LAS v2u*)(smem + L_QB + (2 * w + (lane >> 5)) * 272 + (lane & 31) * 8) = v2u{pk2(a0, a1), pk2(a2, a3)};
    }
    lds_signal(smem, F_QB, w, lane);
    TS(2);
    v4u KF[8];
#pragma unroll
    for (int T = 0; T < 2; T++)
#pragma unroll
      for (int t = 0; t < 4; t++) KF[4 * T + t] = *(const LAS v4u*)(kst + (16 * T + c16) * 256 + (((4 * t + kq) ^ c16) * 16));
    PIN8(KF);
    lds_wait4(smem, F_QB);
    f32x4 S0 = z4, S1 = z4;
#pragma unroll
    for (int t = 0; t < 4; t++) {
      const v4u qf = *(const LAS v4u*)(smem + L_QB + (c16 & 7) * 272 + t * 64 + kq * 16);
      S0 = mfma(KF[t], qf, S0);
      S1 = mfma(KF[4 + t], qf, S1);
    }
    constexpr float C2 = 0.08838834764831845f * 1.4426950408889634f;
    float sv[8] = {S0[0] * C2, S0[1] * C2, S0[2] * C2, S0[3] * C2, S1[0] * C2, S1[1] * C2, S1[2] * C2, S1[3] * C2};
    float m = fmaxf(fmaxf(fmaxf(sv[0], sv[1]), fmaxf(sv[2], sv[3])), fmaxf(fmaxf(sv[4], sv[5]), fmaxf(sv[6], sv[7])));
    m = xrow_max(m);
    float l = 0.f;
#pragma unroll
    for (int i = 0; i < 8; i++) { sv[i] = __builtin_amdgcn_exp2f(sv[i] - m); l += sv[i]; }
    l = xrow_sum(l);
    v4u pf = {pk2(sv[0], sv[1]), pk2(sv[2], sv[3]), pk2(sv[4], sv[5]), pk2(sv[6], sv[7])};
    if (lane < 8) *(LAS v2u*)(smem + L_ML + (w * 8 + lane) * 8) = v2u{__float_as_uint(m), __float_as_uint(l)};
    asm volatile("" : "+v"(pf));
    TS(3);
    v4u VF[8];
    transpose8(VR, VF);
    f32x4 O[8];
#pragma unroll
    for (int j = 0; j < 8; j++) O[j] = mfma(VF[j], pf, z4);
    PIN8(O);
    if (c16 < 8) {
#pragma unroll
      for (int j = 0; j < 8; j++) *(LAS f32x4*)(smem + L_AO + (w * 8 + j) * 528 + kq * 128 + c16 * 16) = O[j];
    }
    lds_signal(smem, F_AT, w, lane);
    TS(4);
    lds_wait4(smem, F_AT);
    {
      const int hh = 2 * w + (lane >> 5), q4 = (lane >> 3) & 3, j = lane & 7;
      float mw[4], lw[4];
      f32x4 ow[4];
#pragma unroll
      for (int ww = 0; ww < 4; ww++) {
        const v2u t = *(const LAS v2u*)(smem + L_ML + (ww * 8 + hh) * 8); mw[ww] = __uint_as_float(t[0]); lw[ww] = __uint_as_float(t[1]);
        ow[ww] = *(const LAS f32x4*)(smem + L_AO + (ww * 8 + j) * 528 + q4 * 128 + hh * 16);
      }
      const float M = fmaxf(fmaxf(mw[0], mw[1]), fmaxf(mw[2], mw[3]));
      float L = 0.f; f32x4 a = z4;
#pragma unroll
      for (int ww = 0; ww < 4; ww++) {
        const float e = __builtin_amdgcn_exp2f(mw[ww] - M);
        L += e * lw[ww];
        a += e * ow[ww];
      }
      const float ri = __builtin_amdgcn_rcpf(L);
      const v4u o = {pk2(a[0] * ri, a[1] * ri), tag, pk2(a[2] * ri, a[3] * ri), tag};
      const int h = g * 8 + hh;
      st_sc1(rs, WS_OP + ((h * 4 + q4) * 4 + b) * 2048 + sp * 128 + j * 16, o);
      if ((lane & 31) == 0) {
        const float lse = M + __builtin_amdgcn_logf(L);
        __builtin_amdgcn_raw_buffer_store_b64(v2u{__float_as_uint(lse), tag}, rs, WS_LS + ((h * 4 + b) * 16 + sp) * 8, 0, AUX_SC1);
      }
    }
    TS(5);
    {
      const int sl = lane >> 3, j = lane & 7;
      int off = WS_OP + ((h2 * 4 + u2) * 4 + w) * 2048 + sl * 256 + j * 16;
      const int loff = WS_LS + ((h2 * 4 + w) * 16 + 2 * sl) * 8;
      v4u o0, o1, ls;
#pragma unroll 1
      for (unsigned it = 0; it < GSPIN; it++) {
        __builtin_amdgcn_s_sleep(2);
        OPAQUE(off);
        o0 = ld_sc1(rs, off); o1 = ld_sc1(rs, off + 128); ls = ld_sc1(rs, loff);
        const bool ok = (o0[1] == tag) & (o0[3] == tag) & (o1[1] == tag) & (o1[3] == tag) & (ls[1] == tag) & (ls[3] == tag);
        if (__all(ok)) break;
      }
      TS(6);
      const float l0 = __uint_as_float(ls[0]), l1 = __uint_as_float(ls[2]);
      float M = fmaxf(l0, l1); M = fmaxf(M, dpp<ROR8>(M)); M = xrow_max(M);
      const float e0 = __builtin_amdgcn_exp2f(l0 - M), e1 = __builtin_amdgcn_exp2f(l1 - M);
      float a[5] = {e0 * bflo(o0[0]) + e1 * bflo(o1[0]), e0 * bfhi(o0[0]) + e1 * bfhi(o1[0]), e0 * bflo(o0[2]) + e1 * bflo(o1[2]), e0 * bfhi(o0[2]) + e1 * bfhi(o1[2]), e0 + e1};
#pragma unroll
      for (int r = 0; r < 5; r++) { a[r] += dpp<ROR8>(a[r]); a[r] = xrow_sum(a[r]); }
      const float ri = __builtin_amdgcn_rcpf(a[4]);
      if (lane < 8) *(LAS v2u*)(smem + L_OB + w * 64 + lane * 8) = v2u{pk2(a[0] * ri, a[1] * ri), pk2(a[2] * ri, a[3] * ri)};
      lds_signal(smem, F_O, w, lane);
    }
    TS(7);
  } else {
    const int w = wave - 8;
    if (w == 0 && lane < 34) *(LAS unsigned*)(smem + L_FLAG + lane * 4) = 0u;
    asm volatile("s_waitcnt lgkmcnt(0)" ::: "memory");
    __builtin_amdgcn_s_barrier();
    const int rb = wg >> 6, rcol = (wg & 63) * 16 + w * 4;
    const v2u xraw = *(const GAS v2u*)((const GAS unsigned char*)x_ + rb * 2048 + rcol * 2);
    if (w == 0) {
      const unsigned epoch = __builtin_amdgcn_raw_buffer_load_b32(rs, WS_EPOCH, 0, AUX_SC1);
      if (lane == 0) *(LAS v2u*)(smem + L_TAG) = v2u{epoch + 1u, 1u};
    }
    lds_wait4(smem, F_ISS);
    v4u FD[8], FO[8];
    {
      const __amdgpu_buffer_rsrc_t rd = __builtin_amdgcn_make_buffer_rsrc((void*)wd_, 0, has_wd ? FF * DM * 2 : 0, 0x00020000);
      const __amdgpu_buffer_rsrc_t ro = __builtin_amdgcn_make_buffer_rsrc((void*)wo_, 0, NQ * DM * 2, 0x00020000);
      const int cofs = half * 1024 + w * 256 + c16 * 16;
      const int offd = (32 * v2 + 8 * kq) * (DM * 2) + cofs;
      const int offo = (128 * h2 + 32 * u2 + 2 * kq) * (DM * 2) + cofs;
      v4u R[16];
#pragma unroll
      for (int i = 0; i < 16 + W2THR; i++) {
        if (i < 8) R[i] = __builtin_amdgcn_raw_buffer_load_b128(rd, offd + i * (DM * 2), 0, USE_NT ? 2 : 0);
        else if (i < 16) R[i] = __builtin_amdgcn_raw_buffer_load_b128(ro, offo + (8 * (i & 3) + ((i >> 2) & 1)) * (DM * 2), 0, USE_NT ? 2 : 0);
        SB();
        const int k = i - W2THR;
        if (k >= 0 && (k & 1)) {
          if (k < 8) { tpair(R[k - 1], R[k], FD, k >> 1); }
          else { tpair(R[k - 1], R[k], FO, (k - 8) >> 1); }
          if (k < 8) asm volatile("" :: "v"(FD[0][(k >> 1) & 3]), "v"(FD[7][(k >> 1) & 3])); else asm volatile("" :: "v"(FO[0][((k - 8) >> 1) & 3]), "v"(FO[7][((k - 8) >> 1) & 3]));
          SB();
        }
        if (i == 15) { TS(1); }
      }
    }
    PIN8(FD); PIN8(FO);
    __builtin_amdgcn_s_setprio(3);
    TS(2);
    f32x4 acc[8];
    {
      if (has_wd) lds_wait4(smem, F_H);
      v4u af = *(const LAS v4u*)(smem + L_HB + (c16 >> 2) * 64 + kq * 16);
      if (!has_wd) af = v4u{0u, 0u, 0u, 0u};
#pragma unroll
      for (int j = 0; j < 8; j++) acc[j] = mfma(af, FD[j], z4);
    }
    const unsigned tag = get_tag(smem);
    TS(3);
    {
      lds_wait4(smem, F_O);
      const v4u af = *(const LAS v4u*)(smem + L_OB + (c16 >> 2) * 64 + kq * 16);
#pragma unroll
      for (int j = 0; j < 8; j++) acc[j] = mfma(af, FO[j], acc[j]);
    }
    {
      const int cg = half * 32 + w * 8 + (c16 >> 1);
      const int off = WS_OUT + (kq * 64 + cg) * 8192 + (h2 * 4 + u2) * 64 + (c16 & 1) * 32;
      st_sc1(rs, off, v4u{pk2(acc[0][0], acc[1][0]), tag, pk2(acc[2][0], acc[3][0]), tag});
      st_sc1(rs, off + 16, v4u{pk2(acc[4][0], acc[5][0]), tag, pk2(acc[6][0], acc[7][0]), tag});
    }
    TS(4);
    {
      int off = WS_OUT + wg * 8192 + lane * 64 + w * 16;
      v4u p0, p1;
#pragma unroll 1
      for (unsigned it = 0; it < GSPIN; it++) {
        OPAQUE(off);
        p0 = ld_sc1(rs, off); p1 = ld_sc1(rs, off + 4096);
        const bool ok = (p0[1] == tag) & (p0[3] == tag) & (p1[1] == tag) & (p1[3] == tag);
        if (__all(ok)) break;
        __builtin_amdgcn_s_sleep(1);
      }
      TS(5);
      float a[4] = {bflo(p0[0]) + bflo(p1[0]), bfhi(p0[0]) + bfhi(p1[0]), bflo(p0[2]) + bflo(p1[2]), bfhi(p0[2]) + bfhi(p1[2])};
#pragma unroll
      for (int r = 0; r < 4; r++) a[r] = wave_sum(a[r]);
      if (lane == 0) {
        const v2u o = {pk2(a[0] + bflo(xraw[0]), a[1] + bfhi(xraw[0])), pk2(a[2] + bflo(xraw[1]), a[3] + bfhi(xraw[1]))};
        *(GAS v2u*)((GAS unsigned char*)out_ + rb * 2048 + rcol * 2) = o;
        if (wg == 0 && w == 0) *(GAS unsigned*)(ws + WS_EPOCH) = tag;
      }
    }
    TS(6);
  }
  TS_OUT();
}

extern "C" void kernel_launch(void* const* d_in, const int* in_sizes, int n_in, void* d_out, int out_size, void* d_ws, size_t ws_size,
                              hipStream_t stream) {
  fused_decode<<<NWG, NTHR, 0, stream>>>((const u16*)d_in[0], (const u16*)d_in[1], (const u16*)d_in[2], (const u16*)d_in[3], (const u16*)d_in[4],
                                         (const u16*)d_in[6], (const u16*)d_in[7], (const u16*)d_in[8], (u16*)d_out, (unsigned char*)d_ws);
}
```

```cpp
#include <hip/hip_runtime.h>
#include <stdint.h>

#define USE_NT 1
#define STAMP 1
#define NOPOLL 0
#define W2THR 4

#define GAS __attribute__((address_space(1)))
#define LAS __attribute__((address_space(3)))
typedef unsigned v4u __attribute__((ext_vector_type(4)));
typedef unsigned v2u __attribute__((ext_vector_type(2)));
typedef float f32x4 __attribute__((ext_vector_type(4)));
typedef __bf16 bf16x8 __attribute__((ext_vector_type(8)));
typedef unsigned short u16;

constexpr int DM = 1024, NQ = 4096, SK = 8192, DH = 128, WIN = 2048, FF = 3584;
constexpr int NWG = 256, NTHR = 768;

constexpr int WS_EPOCH = 0;
constexpr int WS_QP = 4096;
constexpr int WS_GU = WS_QP + 524288;
constexpr int WS_OP = WS_GU + 917504;
constexpr int WS_LS = WS_OP + 1048576;
constexpr int WS_OUT = WS_LS + 16384;
constexpr int WS_END = WS_OUT + 2097152;
constexpr int WS_STAMP = 8 << 20;

constexpr int L_FLAG = 0;
constexpr int L_TAG = 128;
constexpr int L_XN = 256;
constexpr int L_RED = L_XN + 8192;
constexpr int L_KST = L_RED + 24576;
constexpr int L_QB = L_KST + 32768;
constexpr int L_AO = L_QB + 2304;
constexpr int L_ML = L_AO + 4 * 8 * 528;
constexpr int L_OB = L_ML + 256;
constexpr int L_HB = L_OB + 256;
constexpr int L_END = L_HB + 256;
static_assert(L_END <= 160 * 1024 && L_END > 80 * 1024, "lds");
enum { F_XN = 0, F_Q, F_GU, F_QB, F_AT, F_O, F_H, F_ISS };

constexpr int AUX_SC1 = 16;
constexpr unsigned GSPIN = NOPOLL ? 1u : (1u << 14), LSPIN = 1u << 18;

__device__ __forceinline__ v4u ldw(const GAS void* p) {
  return __builtin_nontemporal_load((const GAS v4u*)p);
}
__device__ __forceinline__ float bflo(unsigned v) { return __uint_as_float(v << 16); }
__device__ __forceinline__ float bfhi(unsigned v) { return __uint_as_float(v & 0xffff0000u); }
__device__ __forceinline__ unsigned pk2(float lo, float hi) {
  typedef __bf16 bf2 __attribute__((ext_vector_type(2)));
  bf2 r; r.x = (__bf16)lo; r.y = (__bf16)hi;
  return __builtin_bit_cast(unsigned, r);
}
__device__ __forceinline__ float rbf(float f) { return (float)(__bf16)f; }
template <int CTRL> __device__ __forceinline__ float dpp(float x) {
  return __builtin_bit_cast(float, __builtin_amdgcn_mov_dpp(__builtin_bit_cast(int, x), CTRL, 0xf, 0xf, true));
}
constexpr int XOR1 = 0xB1, XOR2 = 0x4E, HMIR = 0x141, ROR8 = 0x128, ROR4 = 0x124;
__device__ __forceinline__ float xrow_sum(float x) {
  auto s = __builtin_amdgcn_permlane16_swap(__float_as_uint(x), __float_as_uint(x), false, false);
  x = __uint_as_float(s[0]) + __uint_as_float(s[1]);
  auto t = __builtin_amdgcn_permlane32_swap(__float_as_uint(x), __float_as_uint(x), false, false);
  return __uint_as_float(t[0]) + __uint_as_float(t[1]);
}
__device__ __forceinline__ float xrow_max(float x) {
  auto s = __builtin_amdgcn_permlane16_swap(__float_as_uint(x), __float_as_uint(x), false, false);
  x = fmaxf(__uint_as_float(s[0]), __uint_as_float(s[1]));
  auto t = __builtin_amdgcn_permlane32_swap(__float_as_uint(x), __float_as_uint(x), false, false);
  return fmaxf(__uint_as_float(t[0]), __uint_as_float(t[1]));
}
__device__ __forceinline__ float wave_sum(float x) {
  x += dpp<XOR1>(x); x += dpp<XOR2>(x); x += dpp<HMIR>(x); x += dpp<ROR8>(x);
  return xrow_sum(x);
}
__device__ __forceinline__ f32x4 mfma(v4u a, v4u b, f32x4 c) {
  return __builtin_amdgcn_mfma_f32_16x16x32_bf16(__builtin_bit_cast(bf16x8, a), __builtin_bit_cast(bf16x8, b), c, 0, 0, 0);
}
__device__ __forceinline__ void tpair(const v4u& r0, const v4u& r1, v4u (&F)[8], int m) {
#pragma unroll
  for (int d = 0; d < 4; d++) {
    F[2 * d][m] = __builtin_amdgcn_perm(r1[d], r0[d], 0x05040100u);
    F[2 * d + 1][m] = __builtin_amdgcn_perm(r1[d], r0[d], 0x07060302u);
  }
}
__device__ __forceinline__ void transpose8(const v4u (&R)[8], v4u (&F)[8]) {
#pragma unroll
  for (int m = 0; m < 4; m++) tpair(R[2 * m], R[2 * m + 1], F, m);
}
#define SB() __builtin_amdgcn_sched_barrier(0)
#define PIN8(F) asm volatile("" : "+v"(F[0]), "+v"(F[1]), "+v"(F[2]), "+v"(F[3]), "+v"(F[4]), "+v"(F[5]), "+v"(F[6]), "+v"(F[7]))
#define OPAQUE(x) asm volatile("" : "+v"(x) :: "memory")
__device__ __forceinline__ void lds_signal(LAS unsigned char* smem, int f, int w, int lane) {
  asm volatile("s_waitcnt lgkmcnt(0)" ::: "memory");
  if (lane == 0) *(volatile LAS unsigned*)(smem + L_FLAG + f * 16 + w * 4) = 1u;
}
__device__ __forceinline__ void lds_wait4(LAS unsigned char* smem, int f) {
#pragma unroll 1
  for (unsigned i = 0; i < LSPIN; i++) {
    const v4u v = *(volatile LAS v4u*)(smem + L_FLAG + f * 16);
    if (__builtin_amdgcn_readfirstlane(v[0] & v[1] & v[2] & v[3])) break;
  }
  asm volatile("" ::: "memory");
}
__device__ __forceinline__ unsigned get_tag(LAS unsigned char* smem) {
  v2u v;
#pragma unroll 1
  for (unsigned i = 0; i < LSPIN; i++) {
    v = *(volatile LAS v2u*)(smem + L_TAG);
    if (__builtin_amdgcn_readfirstlane(v[1])) break;
  }
  return __builtin_amdgcn_readfirstlane(v[0]);
}
__device__ __forceinline__ v4u ld_sc1(__amdgpu_buffer_rsrc_t rs, int off) {
  return __builtin_amdgcn_raw_buffer_load_b128(rs, off, 0, AUX_SC1);
}
__device__ __forceinline__ void st_sc1(__amdgpu_buffer_rsrc_t rs, int off, v4u v) { __builtin_amdgcn_raw_buffer_store_b128(v, rs, off, 0, AUX_SC1); }

#define TS(i) do { SB(); ts[i] = __builtin_amdgcn_s_memrealtime(); SB(); } while (0)
#define TS_DECL unsigned long long ts[8] = {0, 0, 0, 0, 0, 0, 0, 0}
#define TS_OUT() do { if (lane == 0) { GAS unsigned long long* sp = (GAS unsigned long long*)(ws + WS_STAMP) + (size_t)(wg * 12 + wave) * 8; for (int i_ = 0; i_ < 8; i_++) sp[i_] = ts[i_]; } } while (0)

__global__ void __launch_bounds__(NTHR, 3)
fused_decode(const u16* __restrict__ x_, const u16* __restrict__ kc_, const u16* __restrict__ vc_, const u16* __restrict__ wq_,
             const u16* __restrict__ wo_, const u16* __restrict__ wg_, const u16* __restrict__ wu_, const u16* __restrict__ wd_,
             u16* __restrict__ out_, unsigned char* ws_) {
  __shared__ __attribute__((aligned(16))) unsigned char smem_[L_END];
  LAS unsigned char* smem = (LAS unsigned char*)smem_;
  GAS unsigned char* ws = (GAS unsigned char*)ws_;
  const int lane = threadIdx.x & 63;
  const int wave = __builtin_amdgcn_readfirstlane(threadIdx.x >> 6);
  const int wg = blockIdx.x, xc = wg & 7, y = wg >> 3;
  const int c16 = lane & 15, kq = lane >> 4;
  const __amdgpu_buffer_rsrc_t rs = __builtin_amdgcn_make_buffer_rsrc((void*)ws_, 0, WS_END, 0x00020000);
  const int half = (y >> 2) & 1, z = (y & 3) | ((y >> 3) << 2);
  const int h2 = xc * 4 + (z >> 2), u2 = z & 3;
  const bool has_wd = (z != 11) && (z != 15);
  const int v2 = xc * 14 + (z > 11 ? z - 1 : z);
  const f32x4 z4 = {0.f, 0.f, 0.f, 0.f};
  TS_DECL;
  TS(0);

  if (wave < 4) {
    const int w = wave;
    __builtin_amdgcn_s_setprio(3);
    const int ks = xc, hq = y;
    const bool has_f1 = y >= 4;
    const int fb = y >= 30 ? y - 30 : y - 2;
    const GAS unsigned char* xp = (const GAS unsigned char*)x_ + w * 2048 + lane * 16;
    v4u x0 = *(const GAS v4u*)xp, x1 = *(const GAS v4u*)(xp + 1024);
    const int krow = 128 * ks + 32 * w + 8 * kq;
    v4u R[8], G[8], U[8];
    {
      const GAS unsigned char* p = (const GAS unsigned char*)wq_ + (size_t)krow * (NQ * 2) + hq * 256 + c16 * 16;
#pragma unroll
      for (int i = 0; i < 8; i++) R[i] = ldw(p + (size_t)i * (NQ * 2));
    }
    SB();
    __builtin_amdgcn_s_barrier();
    SB();
    const __amdgpu_buffer_rsrc_t rg = __builtin_amdgcn_make_buffer_rsrc((void*)wg_, 0, has_f1 ? DM * FF * 2 : 0, 0x00020000);
    const __amdgpu_buffer_rsrc_t ru = __builtin_amdgcn_make_buffer_rsrc((void*)wu_, 0, has_f1 ? DM * FF * 2 : 0, 0x00020000);
    const int fo = krow * (FF * 2) + fb * 256 + c16 * 16;
    {
      float v[16];
#pragma unroll
      for (int i = 0; i < 4; i++) { v[2 * i] = bflo(x0[i]); v[2 * i + 1] = bfhi(x0[i]); v[8 + 2 * i] = bflo(x1[i]); v[9 + 2 * i] = bfhi(x1[i]); }
      float s = 0.f;
#pragma unroll
      for (int i = 0; i < 16; i++) s += v[i];
      const float mean = wave_sum(s) * (1.0f / DM);
      float q = 0.f;
#pragma unroll
      for (int i = 0; i < 16; i++) { const float d = v[i] - mean; q += d * d; }
      const float var = wave_sum(q) * (1.0f / DM);
      const float mb = rbf(mean);
      const float den = rbf(__builtin_sqrtf(rbf(rbf(var) + 1e-5f)));
      const float rinv = 1.0f / den;
      v4u o0, o1;
#pragma unroll
      for (int i = 0; i < 4; i++) {
        o0[i] = pk2(rbf(v[2 * i] - mb) * rinv, rbf(v[2 * i + 1] - mb) * rinv);
        o1[i] = pk2(rbf(v[8 + 2 * i] - mb) * rinv, rbf(v[9 + 2 * i] - mb) * rinv);
      }
      *(LAS v4u*)(smem + L_XN + w * 2048 + lane * 16) = o0;
      *(LAS v4u*)(smem + L_XN + w * 2048 + 1024 + lane * 16) = o1;
    }
    lds_signal(smem, F_XN, w, lane);
    SB();
#pragma unroll
    for (int i = 0; i < 8; i++) G[i] = __builtin_amdgcn_raw_buffer_load_b128(rg, fo + i * (FF * 2), 0, USE_NT ? 2 : 0);
    SB();
    TS(1);
    lds_wait4(smem, F_XN);
    const v4u xa = *(const LAS v4u*)(smem + L_XN + (c16 >> 2) * 2048 + krow * 2);
    auto tile = [&](const v4u (&W)[8], int mat) {
      v4u F[8];
      transpose8(W, F);
      float d[8];
#pragma unroll
      for (int j = 0; j < 8; j++) d[j] = mfma(xa, F[j], z4)[0];
      LAS f32x4* rp = (LAS f32x4*)(smem + L_RED + w * 6144 + mat * 2048 + kq * 512 + c16 * 32);
      rp[0] = f32x4{d[0], d[1], d[2], d[3]};
      rp[1] = f32x4{d[4], d[5], d[6], d[7]};
    };
    tile(R, 0);
    lds_signal(smem, F_Q, w, lane);
    TS(2);
    const unsigned tag = get_tag(smem);
    lds_wait4(smem, F_Q);
    if (lane < 32) {
      f32x4 a = z4;
#pragma unroll
      for (int ww = 0; ww < 4; ww++) a += *(const LAS f32x4*)(smem + L_RED + ww * 6144 + w * 512 + lane * 16);
      const v4u o = {pk2(a[0], a[1]), tag, pk2(a[2], a[3]), tag};
      st_sc1(rs, WS_QP + ((w * 4 + (hq >> 3)) * 8 + ks) * 4096 + (hq & 7) * 512 + lane * 16, o);
    }
    SB();
    TS(3);
#pragma unroll
    for (int i = 0; i < 8; i++) U[i] = __builtin_amdgcn_raw_buffer_load_b128(ru, fo + i * (FF * 2), 0, USE_NT ? 2 : 0);
    SB();
    if (has_f1) {
      tile(G, 1);
      SB();
      tile(U, 2);
      lds_signal(smem, F_GU, w, lane);
      TS(4);
      lds_wait4(smem, F_GU);
      const int mat = lane >> 5, ch = lane & 31;
      f32x4 a = z4;
#pragma unroll
      for (int ww = 0; ww < 4; ww++) a += *(const LAS f32x4*)(smem + L_RED + ww * 6144 + 2048 + mat * 2048 + w * 512 + ch * 16);
      const v4u o = {pk2(a[0], a[1]), tag, pk2(a[2], a[3]), tag};
      st_sc1(rs, WS_GU + (4 * fb + (ch >> 3)) * 8192 + w * 2048 + mat * 1024 + ks * 128 + (ch & 7) * 16, o);
    }
    TS(5);
    if (has_wd) {
      v4u g, u;
      int off = WS_GU + v2 * 8192 + w * 2048 + lane * 16;
#pragma unroll 1
      for (unsigned it = 0; it < GSPIN; it++) {
        __builtin_amdgcn_s_sleep(8);
        OPAQUE(off);
        g = ld_sc1(rs, off); u = ld_sc1(rs, off + 1024);
        const bool ok = (g[1] == tag) & (g[3] == tag) & (u[1] == tag) & (u[3] == tag);
        if (__all(ok)) break;
      }
      float gv[4] = {bflo(g[0]), bfhi(g[0]), bflo(g[2]), bfhi(g[2])}, uv[4] = {bflo(u[0]), bfhi(u[0]), bflo(u[2]), bfhi(u[2])};
      float hv[4];
#pragma unroll
      for (int r = 0; r < 4; r++) {
        float a = gv[r] + dpp<ROR8>(gv[r]); a = xrow_sum(a);
        float b = uv[r] + dpp<ROR8>(uv[r]); b = xrow_sum(b);
        hv[r] = rbf(a) * rbf(b);
      }
      if (lane < 8) *(LAS v2u*)(smem + L_HB + w * 64 + lane * 8) = v2u{pk2(hv[0], hv[1]), pk2(hv[2], hv[3])};
      lds_signal(smem, F_H, w, lane);
    }
    TS(6);
  } else if (wave < 8) {
    const int w = wave - 4;
    __builtin_amdgcn_s_barrier();
    __builtin_amdgcn_s_setprio(2);
    const int pair = xc * 2 + (y >> 4), sp = y & 15;
    const int b = pair >> 2, g = pair & 3;
    const size_t kv0 = ((size_t)pair * SK + (SK - WIN) + 128 * sp + 32 * w) * (DH * 2);
    v4u KR[8], VR[8], Q[8];
    int qoff = WS_QP + pair * 32768 + w * 1024 + lane * 16;
    lds_wait4(smem, F_Q);
#pragma unroll
    for (int i = 0; i < 8; i++) KR[i] = ldw((const GAS unsigned char*)kc_ + kv0 + i * 1024 + lane * 16);
    SB();
#pragma unroll
    for (int i = 0; i < 8; i++) Q[i] = ld_sc1(rs, qoff + i * 4096);
    SB();
#pragma unroll
    for (int i = 0; i < 8; i++) VR[i] = ldw((const GAS unsigned char*)vc_ + kv0 + (16 * (i >> 2) + 4 * kq + (i & 3)) * 256 + c16 * 16);
    SB();
    __builtin_amdgcn_s_setprio(3);
    lds_signal(smem, F_ISS, w, lane);
    TS(1);
    LAS unsigned char* kst = smem + L_KST + w * 8192;
#pragma unroll
    for (int i = 0; i < 8; i++) { const int key = 4 * i + kq; *(LAS v4u*)(kst + key * 256 + ((c16 ^ (key & 15)) * 16)) = KR[i]; }
    SB();
    const unsigned tag = get_tag(smem);
    {
      bool ok = true;
#pragma unroll
      for (int i = 0; i < 8; i++) ok &= (Q[i][1] == tag) & (Q[i][3] == tag);
      if (!__all(ok)) {
#pragma unroll 1
        for (unsigned it = 0; it < GSPIN; it++) {
          OPAQUE(qoff);
#pragma unroll
          for (int i = 0; i < 8; i++) Q[i] = ld_sc1(rs, qoff + i * 4096);
          ok = true;
#pragma unroll
          for (int i = 0; i < 8; i++) ok &= (Q[i][1] == tag) & (Q[i][3] == tag);
          if (__all(ok)) break;
        }
      }
      float a0 = 0.f, a1 = 0.f, a2 = 0.f, a3 = 0.f;
#pragma unroll
      for (int i = 0; i < 8; i++) { a0 += bflo(Q[i][0]); a1 += bfhi(Q[i][0]); a2 += bflo(Q[i][2]); a3 += bfhi(Q[i][2]); }
      *(LAS v2u*)(smem + L_QB + (2 * w + (lane >> 5)) * 272 + (lane & 31) * 8) = v2u{pk2(a0, a1), pk2(a2, a3)};
    }
    lds_signal(smem, F_QB, w, lane);
    TS(2);
    v4u KF[8];
#pragma unroll
    for (int T = 0; T < 2; T++)
#pragma unroll
      for (int t = 0; t < 4; t++) KF[4 * T + t] = *(const LAS v4u*)(kst + (16 * T + c16) * 256 + (((4 * t + kq) ^ c16) * 16));
    PIN8(KF);
    lds_wait4(smem, F_QB);
    f32x4 S0 = z4, S1 = z4;
#pragma unroll
    for (int t = 0; t < 4; t++) {
      const v4u qf = *(const LAS v4u*)(smem + L_QB + (c16 & 7) * 272 + t * 64 + kq * 16);
      S0 = mfma(KF[t], qf, S0);
      S1 = mfma(KF[4 + t], qf, S1);
    }
    constexpr float C2 = 0.08838834764831845f * 1.4426950408889634f;
    float sv[8] = {S0[0] * C2, S0[1] * C2, S0[2] * C2, S0[3] * C2, S1[0] * C2, S1[1] * C2, S1[2] * C2, S1[3] * C2};
    float m = fmaxf(fmaxf(fmaxf(sv[0], sv[1]), fmaxf(sv[2], sv[3])), fmaxf(fmaxf(sv[4], sv[5]), fmaxf(sv[6], sv[7])));
    m = xrow_max(m);
    float l = 0.f;
#pragma unroll
    for (int i = 0; i < 8; i++) { sv[i] = __builtin_amdgcn_exp2f(sv[i] - m); l += sv[i]; }
    l = xrow_sum(l);
    v4u pf = {pk2(sv[0], sv[1]), pk2(sv[2], sv[3]), pk2(sv[4], sv[5]), pk2(sv[6], sv[7])};
    if (lane < 8) *(LAS v2u*)(smem + L_ML + (w * 8 + lane) * 8) = v2u{__float_as_uint(m), __float_as_uint(l)};
    asm volatile("" : "+v"(pf));
    TS(3);
    v4u VF[8];
    transpose8(VR, VF);
    f32x4 O[8];
#pragma unroll
    for (int j = 0; j < 8; j++) O[j] = mfma(VF[j], pf, z4);
    PIN8(O);
    if (c16 < 8) {
#pragma unroll
      for (int j = 0; j < 8; j++) *(LAS f32x4*)(smem + L_AO + (w * 8 + j) * 528 + kq * 128 + c16 * 16) = O[j];
    }
    lds_signal(smem, F_AT, w, lane);
    TS(4);
    lds_wait4(smem, F_AT);
    {
      const int hh = 2 * w + (lane >> 5), q4 = (lane >> 3) & 3, j = lane & 7;
      float mw[4], lw[4];
      f32x4 ow[4];
#pragma unroll
      for (int ww = 0; ww < 4; ww++) {
        const v2u t = *(const LAS v2u*)(smem + L_ML + (ww * 8 + hh) * 8); mw[ww] = __uint_as_float(t[0]); lw[ww] = __uint_as_float(t[1]);
        ow[ww] = *(const LAS f32x4*)(smem + L_AO + (ww * 8 + j) * 528 + q4 * 128 + hh * 16);
      }
      const float M = fmaxf(fmaxf(mw[0], mw[1]), fmaxf(mw[2], mw[3]));
      float L = 0.f; f32x4 a = z4;
#pragma unroll
      for (int ww = 0; ww < 4; ww++) {
        const float e = __builtin_amdgcn_exp2f(mw[ww] - M);
        L += e * lw[ww];
        a += e * ow[ww];
      }
      const float ri = __builtin_amdgcn_rcpf(L);
      const v4u o = {pk2(a[0] * ri, a[1] * ri), tag, pk2(a[2] * ri, a[3] * ri), tag};
      const int h = g * 8 + hh;
      st_sc1(rs, WS_OP + ((h * 4 + q4) * 4 + b) * 2048 + sp * 128 + j * 16, o);
      if ((lane & 31) == 0) {
        const float lse = M + __builtin_amdgcn_logf(L);
        __builtin_amdgcn_raw_buffer_store_b64(v2u{__float_as_uint(lse), tag}, rs, WS_LS + ((h * 4 + b) * 16 + sp) * 8, 0, AUX_SC1);
      }
    }
    TS(5);
    {
      const int sl = lane >> 3, j = lane & 7;
      int off = WS_OP + ((h2 * 4 + u2) * 4 + w) * 2048 + sl * 256 + j * 16;
      const int loff = WS_LS + ((h2 * 4 + w) * 16 + 2 * sl) * 8;
      v4u o0, o1, ls;
#pragma unroll 1
      for (unsigned it = 0; it < GSPIN; it++) {
        __builtin_amdgcn_s_sleep(2);
        OPAQUE(off);
        o0 = ld_sc1(rs, off); o1 = ld_sc1(rs, off + 128); ls = ld_sc1(rs, loff);
        const bool ok = (o0[1] == tag) & (o0[3] == tag) & (o1[1] == tag) & (o1[3] == tag) & (ls[1] == tag) & (ls[3] == tag);
        if (__all(ok)) break;
      }
      TS(6);
      const float l0 = __uint_as_float(ls[0]), l1 = __uint_as_float(ls[2]);
      float M = fmaxf(l0, l1); M = fmaxf(M, dpp<ROR8>(M)); M = xrow_max(M);
      const float e0 = __builtin_amdgcn_exp2f(l0 - M), e1 = __builtin_amdgcn_exp2f(l1 - M);
      float a[5] = {e0 * bflo(o0[0]) + e1 * bflo(o1[0]), e0 * bfhi(o0[0]) + e1 * bfhi(o1[0]), e0 * bflo(o0[2]) + e1 * bflo(o1[2]), e0 * bfhi(o0[2]) + e1 * bfhi(o1[2]), e0 + e1};
#pragma unroll
      for (int r = 0; r < 5; r++) { a[r] += dpp<ROR8>(a[r]); a[r] = xrow_sum(a[r]); }
      const float ri = __builtin_amdgcn_rcpf(a[4]);
      if (lane < 8) *(LAS v2u*)(smem + L_OB + w * 64 + lane * 8) = v2u{pk2(a[0] * ri, a[1] * ri), pk2(a[2] * ri, a[3] * ri)};
      lds_signal(smem, F_O, w, lane);
    }
    TS(7);
  } else {
    const int w = wave - 8;
    if (w == 0 && lane < 34) *(LAS unsigned*)(smem + L_FLAG + lane * 4) = 0u;
    asm volatile("s_waitcnt lgkmcnt(0)" ::: "memory");
    __builtin_amdgcn_s_barrier();
    const int rb = wg >> 6, rcol = (wg & 63) * 16 + w * 4;
    const v2u xraw = *(const GAS v2u*)((const GAS unsigned char*)x_ + rb * 2048 + rcol * 2);
    if (w == 0) {
      const unsigned epoch = __builtin_amdgcn_raw_buffer_load_b32(rs, WS_EPOCH, 0, AUX_SC1);
      if (lane == 0) *(LAS v2u*)(smem + L_TAG) = v2u{epoch + 1u, 1u};
    }
    lds_wait4(smem, F_ISS);
    v4u FD[8], FO[8];
    {
      const __amdgpu_buffer_rsrc_t rd = __builtin_amdgcn_make_buffer_rsrc((void*)wd_, 0, has_wd ? FF * DM * 2 : 0, 0x00020000);
      const __amdgpu_buffer_rsrc_t ro = __builtin_amdgcn_make_buffer_rsrc((void*)wo_, 0, NQ * DM * 2, 0x00020000);
      const int cofs = half * 1024 + w * 256 + c16 * 16;
      const int offd = (32 * v2 + 8 * kq) * (DM * 2) + cofs;
      const int offo = (128 * h2 + 32 * u2 + 2 * kq) * (DM * 2) + cofs;
      v4u R[16];
#pragma unroll
      for (int i = 0; i < 16 + W2THR; i++) {
        if (i < 8) R[i] = __builtin_amdgcn_raw_buffer_load_b128(rd, offd + i * (DM * 2), 0, USE_NT ? 2 : 0);
        else if (i < 16) R[i] = __builtin_amdgcn_raw_buffer_load_b128(ro, offo + (8 * (i & 3) + ((i >> 2) & 1)) * (DM * 2), 0, USE_NT ? 2 : 0);
        SB();
        const int k = i - W2THR;
        if (k >= 0 && (k & 1)) {
          if (k < 8) { tpair(R[k - 1], R[k], FD, k >> 1); }
          else { tpair(R[k - 1], R[k], FO, (k - 8) >> 1); }
          if (k < 8) asm volatile("" :: "v"(FD[0][(k >> 1) & 3]), "v"(FD[7][(k >> 1) & 3])); else asm volatile("" :: "v"(FO[0][((k - 8) >> 1) & 3]), "v"(FO[7][((k - 8) >> 1) & 3]));
          SB();
        }
        if (i == 15) { TS(1); }
      }
    }
    PIN8(FD); PIN8(FO);
    __builtin_amdgcn_s_setprio(3);
    TS(2);
    f32x4 acc[8];
    {
      if (has_wd) lds_wait4(smem, F_H);
      v4u af = *(const LAS v4u*)(smem + L_HB + (c16 >> 2) * 64 + kq * 16);
      if (!has_wd) af = v4u{0u, 0u, 0u, 0u};
#pragma unroll
      for (int j = 0; j < 8; j++) acc[j] = mfma(af, FD[j], z4);
    }
    const unsigned tag = get_tag(smem);
    TS(3);
    {
      lds_wait4(smem, F_O);
      const v4u af = *(const LAS v4u*)(smem + L_OB + (c16 >> 2) * 64 + kq * 16);
#pragma unroll
      for (int j = 0; j < 8; j++) acc[j] = mfma(af, FO[j], acc[j]);
    }
    {
      const int cg = half * 32 + w * 8 + (c16 >> 1);
      const int off = WS_OUT + (kq * 64 + cg) * 8192 + (h2 * 4 + u2) * 64 + (c16 & 1) * 32;
      st_sc1(rs, off, v4u{pk2(acc[0][0], acc[1][0]), tag, pk2(acc[2][0], acc[3][0]), tag});
      st_sc1(rs, off + 16, v4u{pk2(acc[4][0], acc[5][0]), tag, pk2(acc[6][0], acc[7][0]), tag});
    }
    TS(4);
    {
      int off = WS_OUT + wg * 8192 + lane * 64 + w * 16;
      v4u p0, p1;
#pragma unroll 1
      for (unsigned it = 0; it < GSPIN; it++) {
        OPAQUE(off);
        p0 = ld_sc1(rs, off); p1 = ld_sc1(rs, off + 4096);
        const bool ok = (p0[1] == tag) & (p0[3] == tag) & (p1[1] == tag) & (p1[3] == tag);
        if (__all(ok)) break;
        __builtin_amdgcn_s_sleep(1);
      }
      TS(5);
      float a[4] = {bflo(p0[0]) + bflo(p1[0]), bfhi(p0[0]) + bfhi(p1[0]), bflo(p0[2]) + bflo(p1[2]), bfhi(p0[2]) + bfhi(p1[2])};
#pragma unroll
      for (int r = 0; r < 4; r++) a[r] = wave_sum(a[r]);
      if (lane == 0) {
        const v2u o = {pk2(a[0] + bflo(xraw[0]), a[1] + bfhi(xraw[0])), pk2(a[2] + bflo(xraw[1]), a[3] + bfhi(xraw[1]))};
        *(GAS v2u*)((GAS unsigned char*)out_ + rb * 2048 + rcol * 2) = o;
        if (wg == 0 && w == 0) *(GAS unsigned*)(ws + WS_EPOCH) = tag;
      }
    }
    TS(6);
  }
  TS_OUT();
}

#include <cstdio>
#include <vector>
#include <algorithm>
__global__ void k_fill(unsigned* p, size_t n, unsigned v) { size_t i = (size_t)blockIdx.x * blockDim.x + threadIdx.x; size_t st = (size_t)gridDim.x * blockDim.x; for (; i < n; i += st) { unsigned x = (unsigned)i * 2654435761u + v * 40503u; x ^= x >> 15; x *= 0x85EBCA77u; x ^= x >> 13; p[i] = x; } }
extern "C" void kernel_launch(void* const* d_in, const int* in_sizes, int n_in, void* d_out, int out_size, void* d_ws, size_t ws_size,
                              hipStream_t stream) {
  auto launch = [&]() { fused_decode<<<NWG, NTHR, 0, stream>>>((const u16*)d_in[0], (const u16*)d_in[1], (const u16*)d_in[2], (const u16*)d_in[3], (const u16*)d_in[4],
                                         (const u16*)d_in[6], (const u16*)d_in[7], (const u16*)d_in[8], (u16*)d_out, (unsigned char*)d_ws); };
  hipStreamCaptureStatus cs; hipStreamIsCapturing(stream, &cs);
  fprintf(stderr, "DIAG capturing=%d\n", (int)cs);
  for (int i = 0; i < n_in + 2; i++) {
    void* p = i < n_in ? d_in[i] : (i == n_in ? d_out : d_ws);
    hipPointerAttribute_t at; hipError_t e = hipPointerGetAttributes(&at, p);
    hipDeviceptr_t base = 0; size_t sz = 0; hipError_t e2 = hipMemGetAddressRange(&base, &sz, p);
    fprintf(stderr, "DIAG buf %d ptr %p attr(err %d type %d managed %d flags %x) range(err %d base %p size %zu)\n", i, p, (int)e, (int)at.type, (int)at.isManaged, at.allocationFlags, (int)e2, (void*)base, sz);
  }
  launch();
  if (cs == hipStreamCaptureStatusNone) {
    std::vector<unsigned long long> hs(256 * 12 * 8);
    for (int it = 0; it < 6; it++) {
      hipStreamSynchronize(stream);
      hipMemcpy(hs.data(), (char*)d_ws + WS_STAMP, hs.size() * 8, hipMemcpyDeviceToHost);
      unsigned long long t0 = ~0ull, t1 = 0; for (int i = 0; i < 256 * 12; i++) { t0 = std::min(t0, hs[i * 8]); for (int k = 0; k < 8; k++) t1 = std::max(t1, hs[i * 8 + k]); }
      fprintf(stderr, "DIAG it %d span %.2f :", it, (t1 - t0) * 0.01);
      for (int wv = 0; wv < 12; wv += 4) { for (int k = 0; k < 8; k++) { std::vector<double> v; for (int g = 0; g < 256; g++) { unsigned long long t = hs[(g * 12 + wv) * 8 + k]; if (t) v.push_back((t - t0) * 0.01); } if (v.empty()) continue; std::sort(v.begin(), v.end()); fprintf(stderr, " %.2f/%.2f", v[v.size() / 2], v.back()); } fprintf(stderr, " |"); }
      fprintf(stderr, "\n");
      if (it < 3) k_fill<<<2048, 256, 0, stream>>>((unsigned*)d_ws, ws_size / 4, it + 1);
      hipStreamSynchronize(stream);
      launch();
    }
  }
}
```

```cpp
#define PWG 100
#define PWAVE 0
#define PIDX 1
#define PAMP 0ull
#include <hip/hip_runtime.h>
#include <stdint.h>

#define USE_NT 1
#define STAMP 1
#define NOPOLL 0
#define W2THR 4

#define GAS __attribute__((address_space(1)))
#define LAS __attribute__((address_space(3)))
typedef unsigned v4u __attribute__((ext_vector_type(4)));
typedef unsigned v2u __attribute__((ext_vector_type(2)));
typedef float f32x4 __attribute__((ext_vector_type(4)));
typedef __bf16 bf16x8 __attribute__((ext_vector_type(8)));
typedef unsigned short u16;

constexpr int DM = 1024, NQ = 4096, SK = 8192, DH = 128, WIN = 2048, FF = 3584;
constexpr int NWG = 256, NTHR = 768;

constexpr int WS_EPOCH = 0;
constexpr int WS_QP = 4096;
constexpr int WS_GU = WS_QP + 524288;
constexpr int WS_OP = WS_GU + 917504;
constexpr int WS_LS = WS_OP + 1048576;
constexpr int WS_OUT = WS_LS + 16384;
constexpr int WS_END = WS_OUT + 2097152;
constexpr int WS_STAMP = 8 << 20;

constexpr int L_FLAG = 0;
constexpr int L_TAG = 128;
constexpr int L_XN = 256;
constexpr int L_RED = L_XN + 8192;
constexpr int L_KST = L_RED + 24576;
constexpr int L_QB = L_KST + 32768;
constexpr int L_AO = L_QB + 2304;
constexpr int L_ML = L_AO + 4 * 8 * 528;
constexpr int L_OB = L_ML + 256;
constexpr int L_HB = L_OB + 256;
constexpr int L_END = L_HB + 256;
static_assert(L_END <= 160 * 1024 && L_END > 80 * 1024, "lds");
enum { F_XN = 0, F_Q, F_GU, F_QB, F_AT, F_O, F_H, F_ISS };

constexpr int AUX_SC1 = 16;
constexpr unsigned GSPIN = NOPOLL ? 1u : (1u << 14), LSPIN = 1u << 18;

__device__ __forceinline__ v4u ldw(const GAS void* p) {
  return __builtin_nontemporal_load((const GAS v4u*)p);
}
__device__ __forceinline__ float bflo(unsigned v) { return __uint_as_float(v << 16); }
__device__ __forceinline__ float bfhi(unsigned v) { return __uint_as_float(v & 0xffff0000u); }
__device__ __forceinline__ unsigned pk2(float lo, float hi) {
  typedef __bf16 bf2 __attribute__((ext_vector_type(2)));
  bf2 r; r.x = (__bf16)lo; r.y = (__bf16)hi;
  return __builtin_bit_cast(unsigned, r);
}
__device__ __forceinline__ float rbf(float f) { return (float)(__bf16)f; }
template <int CTRL> __device__ __forceinline__ float dpp(float x) {
  return __builtin_bit_cast(float, __builtin_amdgcn_mov_dpp(__builtin_bit_cast(int, x), CTRL, 0xf, 0xf, true));
}
constexpr int XOR1 = 0xB1, XOR2 = 0x4E, HMIR = 0x141, ROR8 = 0x128, ROR4 = 0x124;
__device__ __forceinline__ float xrow_sum(float x) {
  auto s = __builtin_amdgcn_permlane16_swap(__float_as_uint(x), __float_as_uint(x), false, false);
  x = __uint_as_float(s[0]) + __uint_as_float(s[1]);
  auto t = __builtin_amdgcn_permlane32_swap(__float_as_uint(x), __float_as_uint(x), false, false);
  return __uint_as_float(t[0]) + __uint_as_float(t[1]);
}
__device__ __forceinline__ float xrow_max(float x) {
  auto s = __builtin_amdgcn_permlane16_swap(__float_as_uint(x), __float_as_uint(x), false, false);
  x = fmaxf(__uint_as_float(s[0]), __uint_as_float(s[1]));
  auto t = __builtin_amdgcn_permlane32_swap(__float_as_uint(x), __float_as_uint(x), false, false);
  return fmaxf(__uint_as_float(t[0]), __uint_as_float(t[1]));
}
__device__ __forceinline__ float wave_sum(float x) {
  x += dpp<XOR1>(x); x += dpp<XOR2>(x); x += dpp<HMIR>(x); x += dpp<ROR8>(x);
  return xrow_sum(x);
}
__device__ __forceinline__ f32x4 mfma(v4u a, v4u b, f32x4 c) {
  return __builtin_amdgcn_mfma_f32_16x16x32_bf16(__builtin_bit_cast(bf16x8, a), __builtin_bit_cast(bf16x8, b), c, 0, 0, 0);
}
__device__ __forceinline__ void tpair(const v4u& r0, const v4u& r1, v4u (&F)[8], int m) {
#pragma unroll
  for (int d = 0; d < 4; d++) {
    F[2 * d][m] = __builtin_amdgcn_perm(r1[d], r0[d], 0x05040100u);
    F[2 * d + 1][m] = __builtin_amdgcn_perm(r1[d], r0[d], 0x07060302u);
  }
}
__device__ __forceinline__ void transpose8(const v4u (&R)[8], v4u (&F)[8]) {
#pragma unroll
  for (int m = 0; m < 4; m++) tpair(R[2 * m], R[2 * m + 1], F, m);
}
#define SB() __builtin_amdgcn_sched_barrier(0)
#define PIN8(F) asm volatile("" : "+v"(F[0]), "+v"(F[1]), "+v"(F[2]), "+v"(F[3]), "+v"(F[4]), "+v"(F[5]), "+v"(F[6]), "+v"(F[7]))
#define OPAQUE(x) asm volatile("" : "+v"(x) :: "memory")
__device__ __forceinline__ void lds_signal(LAS unsigned char* smem, int f, int w, int lane) {
  asm volatile("s_waitcnt lgkmcnt(0)" ::: "memory");
  if (lane == 0) *(volatile LAS unsigned*)(smem + L_FLAG + f * 16 + w * 4) = 1u;
}
__device__ __forceinline__ void lds_wait4(LAS unsigned char* smem, int f) {
#pragma unroll 1
  for (unsigned i = 0; i < LSPIN; i++) {
    const v4u v = *(volatile LAS v4u*)(smem + L_FLAG + f * 16);
    if (__builtin_amdgcn_readfirstlane(v[0] & v[1] & v[2] & v[3])) break;
  }
  asm volatile("" ::: "memory");
}
__device__ __forceinline__ unsigned get_tag(LAS unsigned char* smem) {
  v2u v;
#pragma unroll 1
  for (unsigned i = 0; i < LSPIN; i++) {
    v = *(volatile LAS v2u*)(smem + L_TAG);
    if (__builtin_amdgcn_readfirstlane(v[1])) break;
  }
  return __builtin_amdgcn_readfirstlane(v[0]);
}
__device__ __forceinline__ v4u ld_sc1(__amdgpu_buffer_rsrc_t rs, int off) {
  return __builtin_amdgcn_raw_buffer_load_b128(rs, off, 0, AUX_SC1);
}
__device__ __forceinline__ void st_sc1(__amdgpu_buffer_rsrc_t rs, int off, v4u v) { __builtin_amdgcn_raw_buffer_store_b128(v, rs, off, 0, AUX_SC1); }

#define TS(i) do { SB(); ts[i] = __builtin_amdgcn_s_memrealtime(); SB(); } while (0)
#define TS_DECL unsigned long long ts[8] = {0, 0, 0, 0, 0, 0, 0, 0}
#define TS_OUT() do { if (wg == PWG && wave == PWAVE) { const unsigned long long until = ts[0] + 4000ull + PAMP * (ts[PIDX] - ts[0]); while (__builtin_amdgcn_s_memrealtime() < until) __builtin_amdgcn_s_sleep(2); } } while (0)

__global__ void __launch_bounds__(NTHR, 3)
fused_decode(const u16* __restrict__ x_, const u16* __restrict__ kc_, const u16* __restrict__ vc_, const u16* __restrict__ wq_,
             const u16* __restrict__ wo_, const u16* __restrict__ wg_, const u16* __restrict__ wu_, const u16* __restrict__ wd_,
             u16* __restrict__ out_, unsigned char* ws_) {
  __shared__ __attribute__((aligned(16))) unsigned char smem_[L_END];
  LAS unsigned char* smem = (LAS unsigned char*)smem_;
  GAS unsigned char* ws = (GAS unsigned char*)ws_;
  const int lane = threadIdx.x & 63;
  const int wave = __builtin_amdgcn_readfirstlane(threadIdx.x >> 6);
  const int wg = blockIdx.x, xc = wg & 7, y = wg >> 3;
  const int c16 = lane & 15, kq = lane >> 4;
  const __amdgpu_buffer_rsrc_t rs = __builtin_amdgcn_make_buffer_rsrc((void*)ws_, 0, WS_END, 0x00020000);
  const int half = (y >> 2) & 1, z = (y & 3) | ((y >> 3) << 2);
  const int h2 = xc * 4 + (z >> 2), u2 = z & 3;
  const bool has_wd = (z != 11) && (z != 15);
  const int v2 = xc * 14 + (z > 11 ? z - 1 : z);
  const f32x4 z4 = {0.f, 0.f, 0.f, 0.f};
  TS_DECL;
  TS(0);

  if (wave < 4) {
    const int w = wave;
    __builtin_amdgcn_s_setprio(3);
    const int ks = xc, hq = y;
    const bool has_f1 = y >= 4;
    const int fb = y >= 30 ? y - 30 : y - 2;
    const GAS unsigned char* xp = (const GAS unsigned char*)x_ + w * 2048 + lane * 16;
    v4u x0 = *(const GAS v4u*)xp, x1 = *(const GAS v4u*)(xp + 1024);
    const int krow = 128 * ks + 32 * w + 8 * kq;
    v4u R[8], G[8], U[8];
    {
      const GAS unsigned char* p = (const GAS unsigned char*)wq_ + (size_t)krow * (NQ * 2) + hq * 256 + c16 * 16;
#pragma unroll
      for (int i = 0; i < 8; i++) R[i] = ldw(p + (size_t)i * (NQ * 2));
    }
    SB();
    __builtin_amdgcn_s_barrier();
    SB();
    const __amdgpu_buffer_rsrc_t rg = __builtin_amdgcn_make_buffer_rsrc((void*)wg_, 0, has_f1 ? DM * FF * 2 : 0, 0x00020000);
    const __amdgpu_buffer_rsrc_t ru = __builtin_amdgcn_make_buffer_rsrc((void*)wu_, 0, has_f1 ? DM * FF * 2 : 0, 0x00020000);
    const int fo = krow * (FF * 2) + fb * 256 + c16 * 16;
    {
      float v[16];
#pragma unroll
      for (int i = 0; i < 4; i++) { v[2 * i] = bflo(x0[i]); v[2 * i + 1] = bfhi(x0[i]); v[8 + 2 * i] = bflo(x1[i]); v[9 + 2 * i] = bfhi(x1[i]); }
      float s = 0.f;
#pragma unroll
      for (int i = 0; i < 16; i++) s += v[i];
      const float mean = wave_sum(s) * (1.0f / DM);
      float q = 0.f;
#pragma unroll
      for (int i = 0; i < 16; i++) { const float d = v[i] - mean; q += d * d; }
      const float var = wave_sum(q) * (1.0f / DM);
      const float mb = rbf(mean);
      const float den = rbf(__builtin_sqrtf(rbf(rbf(var) + 1e-5f)));
      const float rinv = 1.0f / den;
      v4u o0, o1;
#pragma unroll
      for (int i = 0; i < 4; i++) {
        o0[i] = pk2(rbf(v[2 * i] - mb) * rinv, rbf(v[2 * i + 1] - mb) * rinv);
        o1[i] = pk2(rbf(v[8 + 2 * i] - mb) * rinv, rbf(v[9 + 2 * i] - mb) * rinv);
      }
      *(LAS v4u*)(smem + L_XN + w * 2048 + lane * 16) = o0;
      *(LAS v4u*)(smem + L_XN + w * 2048 + 1024 + lane * 16) = o1;
    }
    lds_signal(smem, F_XN, w, lane);
    SB();
#pragma unroll
    for (int i = 0; i < 8; i++) G[i] = __builtin_amdgcn_raw_buffer_load_b128(rg, fo + i * (FF * 2), 0, USE_NT ? 2 : 0);
    SB();
    TS(1);
    lds_wait4(smem, F_XN);
    const v4u xa = *(const LAS v4u*)(smem + L_XN + (c16 >> 2) * 2048 + krow * 2);
    auto tile = [&](const v4u (&W)[8], int mat) {
      v4u F[8];
      transpose8(W, F);
      float d[8];
#pragma unroll
      for (int j = 0; j < 8; j++) d[j] = mfma(xa, F[j], z4)[0];
      LAS f32x4* rp = (LAS f32x4*)(smem + L_RED + w * 6144 + mat * 2048 + kq * 512 + c16 * 32);
      rp[0] = f32x4{d[0], d[1], d[2], d[3]};
      rp[1] = f32x4{d[4], d[5], d[6], d[7]};
    };
    tile(R, 0);
    lds_signal(smem, F_Q, w, lane);
    TS(2);
    const unsigned tag = get_tag(smem);
    lds_wait4(smem, F_Q);
    if (lane < 32) {
      f32x4 a = z4;
#pragma unroll
      for (int ww = 0; ww < 4; ww++) a += *(const LAS f32x4*)(smem + L_RED + ww * 6144 + w * 512 + lane * 16);
      const v4u o = {pk2(a[0], a[1]), tag, pk2(a[2], a[3]), tag};
      st_sc1(rs, WS_QP + ((w * 4 + (hq >> 3)) * 8 + ks) * 4096 + (hq & 7) * 512 + lane * 16, o);
    }
    SB();
    TS(3);
#pragma unroll
    for (int i = 0; i < 8; i++) U[i] = __builtin_amdgcn_raw_buffer_load_b128(ru, fo + i * (FF * 2), 0, USE_NT ? 2 : 0);
    SB();
    if (has_f1) {
      tile(G, 1);
      SB();
      tile(U, 2);
      lds_signal(smem, F_GU, w, lane);
      TS(4);
      lds_wait4(smem, F_GU);
      const int mat = lane >> 5, ch = lane & 31;
      f32x4 a = z4;
#pragma unroll
      for (int ww = 0; ww < 4; ww++) a += *(const LAS f32x4*)(smem + L_RED + ww * 6144 + 2048 + mat * 2048 + w * 512 + ch * 16);
      const v4u o = {pk2(a[0], a[1]), tag, pk2(a[2], a[3]), tag};
      st_sc1(rs, WS_GU + (4 * fb + (ch >> 3)) * 8192 + w * 2048 + mat * 1024 + ks * 128 + (ch & 7) * 16, o);
    }
    TS(5);
    if (has_wd) {
      v4u g, u;
      int off = WS_GU + v2 * 8192 + w * 2048 + lane * 16;
#pragma unroll 1
      for (unsigned it = 0; it < GSPIN; it++) {
        __builtin_amdgcn_s_sleep(8);
        OPAQUE(off);
        g = ld_sc1(rs, off); u = ld_sc1(rs, off + 1024);
        const bool ok = (g[1] == tag) & (g[3] == tag) & (u[1] == tag) & (u[3] == tag);
        if (__all(ok)) break;
      }
      float gv[4] = {bflo(g[0]), bfhi(g[0]), bflo(g[2]), bfhi(g[2])}, uv[4] = {bflo(u[0]), bfhi(u[0]), bflo(u[2]), bfhi(u[2])};
      float hv[4];
#pragma unroll
      for (int r = 0; r < 4; r++) {
        float a = gv[r] + dpp<ROR8>(gv[r]); a = xrow_sum(a);
        float b = uv[r] + dpp<ROR8>(uv[r]); b = xrow_sum(b);
        hv[r] = rbf(a) * rbf(b);
      }
      if (lane < 8) *(LAS v2u*)(smem + L_HB + w * 64 + lane * 8) = v2u{pk2(hv[0], hv[1]), pk2(hv[2], hv[3])};
      lds_signal(smem, F_H, w, lane);
    }
    TS(6);
  } else if (wave < 8) {
    const int w = wave - 4;
    __builtin_amdgcn_s_barrier();
    __builtin_amdgcn_s_setprio(2);
    const int pair = xc * 2 + (y >> 4), sp = y & 15;
    const int b = pair >> 2, g = pair & 3;
    const size_t kv0 = ((size_t)pair * SK + (SK - WIN) + 128 * sp + 32 * w) * (DH * 2);
    v4u KR[8], VR[8], Q[8];
    int qoff = WS_QP + pair * 32768 + w * 1024 + lane * 16;
    lds_wait4(smem, F_Q);
#pragma unroll
    for (int i = 0; i < 8; i++) KR[i] = ldw((const GAS unsigned char*)kc_ + kv0 + i * 1024 + lane * 16);
    SB();
#pragma unroll
    for (int i = 0; i < 8; i++) Q[i] = ld_sc1(rs, qoff + i * 4096);
    SB();
#pragma unroll
    for (int i = 0; i < 8; i++) VR[i] = ldw((const GAS unsigned char*)vc_ + kv0 + (16 * (i >> 2) + 4 * kq + (i & 3)) * 256 + c16 * 16);
    SB();
    __builtin_amdgcn_s_setprio(3);
    lds_signal(smem, F_ISS, w, lane);
    TS(1);
    LAS unsigned char* kst = smem + L_KST + w * 8192;
#pragma unroll
    for (int i = 0; i < 8; i++) { const int key = 4 * i + kq; *(LAS v4u*)(kst + key * 256 + ((c16 ^ (key & 15)) * 16)) = KR[i]; }
    SB();
    const unsigned tag = get_tag(smem);
    {
      bool ok = true;
#pragma unroll
      for (int i = 0; i < 8; i++) ok &= (Q[i][1] == tag) & (Q[i][3] == tag);
      if (!__all(ok)) {
#pragma unroll 1
        for (unsigned it = 0; it < GSPIN; it++) {
          OPAQUE(qoff);
#pragma unroll
          for (int i = 0; i < 8; i++) Q[i] = ld_sc1(rs, qoff + i * 4096);
          ok = true;
#pragma unroll
          for (int i = 0; i < 8; i++) ok &= (Q[i][1] == tag) & (Q[i][3] == tag);
          if (__all(ok)) break;
        }
      }
      float a0 = 0.f, a1 = 0.f, a2 = 0.f, a3 = 0.f;
#pragma unroll
      for (int i = 0; i < 8; i++) { a0 += bflo(Q[i][0]); a1 += bfhi(Q[i][0]); a2 += bflo(Q[i][2]); a3 += bfhi(Q[i][2]); }
      *(LAS v2u*)(smem + L_QB + (2 * w + (lane >> 5)) * 272 + (lane & 31) * 8) = v2u{pk2(a0, a1), pk2(a2, a3)};
    }
    lds_signal(smem, F_QB, w, lane);
    TS(2);
    v4u KF[8];
#pragma unroll
    for (int T = 0; T < 2; T++)
#pragma unroll
      for (int t = 0; t < 4; t++) KF[4 * T + t] = *(const LAS v4u*)(kst + (16 * T + c16) * 256 + (((4 * t + kq) ^ c16) * 16));
    PIN8(KF);
    lds_wait4(smem, F_QB);
    f32x4 S0 = z4, S1 = z4;
#pragma unroll
    for (int t = 0; t < 4; t++) {
      const v4u qf = *(const LAS v4u*)(smem + L_QB + (c16 & 7) * 272 + t * 64 + kq * 16);
      S0 = mfma(KF[t], qf, S0);
      S1 = mfma(KF[4 + t], qf, S1);
    }
    constexpr float C2 = 0.08838834764831845f * 1.4426950408889634f;
    float sv[8] = {S0[0] * C2, S0[1] * C2, S0[2] * C2, S0[3] * C2, S1[0] * C2, S1[1] * C2, S1[2] * C2, S1[3] * C2};
    float m = fmaxf(fmaxf(fmaxf(sv[0], sv[1]), fmaxf(sv[2], sv[3])), fmaxf(fmaxf(sv[4], sv[5]), fmaxf(sv[6], sv[7])));
    m = xrow_max(m);
    float l = 0.f;
#pragma unroll
    for (int i = 0; i < 8; i++) { sv[i] = __builtin_amdgcn_exp2f(sv[i] - m); l += sv[i]; }
    l = xrow_sum(l);
    v4u pf = {pk2(sv[0], sv[1]), pk2(sv[2], sv[3]), pk2(sv[4], sv[5]), pk2(sv[6], sv[7])};
    if (lane < 8) *(LAS v2u*)(smem + L_ML + (w * 8 + lane) * 8) = v2u{__float_as_uint(m), __float_as_uint(l)};
    asm volatile("" : "+v"(pf));
    TS(3);
    v4u VF[8];
    transpose8(VR, VF);
    f32x4 O[8];
#pragma unroll
    for (int j = 0; j < 8; j++) O[j] = mfma(VF[j], pf, z4);
    PIN8(O);
    if (c16 < 8) {
#pragma unroll
      for (int j = 0; j < 8; j++) *(LAS f32x4*)(smem + L_AO + (w * 8 + j) * 528 + kq * 128 + c16 * 16) = O[j];
    }
    lds_signal(smem, F_AT, w, lane);
    TS(4);
    lds_wait4(smem, F_AT);
    {
      const int hh = 2 * w + (lane >> 5), q4 = (lane >> 3) & 3, j = lane & 7;
      float mw[4], lw[4];
      f32x4 ow[4];
#pragma unroll
      for (int ww = 0; ww < 4; ww++) {
        const v2u t = *(const LAS v2u*)(smem + L_ML + (ww * 8 + hh) * 8); mw[ww] = __uint_as_float(t[0]); lw[ww] = __uint_as_float(t[1]);
        ow[ww] = *(const LAS f32x4*)(smem + L_AO + (ww * 8 + j) * 528 + q4 * 128 + hh * 16);
      }
      const float M = fmaxf(fmaxf(mw[0], mw[1]), fmaxf(mw[2], mw[3]));
      float L = 0.f; f32x4 a = z4;
#pragma unroll
      for (int ww = 0; ww < 4; ww++) {
        const float e = __builtin_amdgcn_exp2f(mw[ww] - M);
        L += e * lw[ww];
        a += e * ow[ww];
      }
      const float ri = __builtin_amdgcn_rcpf(L);
      const v4u o = {pk2(a[0] * ri, a[1] * ri), tag, pk2(a[2] * ri, a[3] * ri), tag};
      const int h = g * 8 + hh;
      st_sc1(rs, WS_OP + ((h * 4 + q4) * 4 + b) * 2048 + sp * 128 + j * 16, o);
      if ((lane & 31) == 0) {
        const float lse = M + __builtin_amdgcn_logf(L);
        __builtin_amdgcn_raw_buffer_store_b64(v2u{__float_as_uint(lse), tag}, rs, WS_LS + ((h * 4 + b) * 16 + sp) * 8, 0, AUX_SC1);
      }
    }
    TS(5);
    {
      const int sl = lane >> 3, j = lane & 7;
      int off = WS_OP + ((h2 * 4 + u2) * 4 + w) * 2048 + sl * 256 + j * 16;
      const int loff = WS_LS + ((h2 * 4 + w) * 16 + 2 * sl) * 8;
      v4u o0, o1, ls;
#pragma unroll 1
      for (unsigned it = 0; it < GSPIN; it++) {
        __builtin_amdgcn_s_sleep(2);
        OPAQUE(off);
        o0 = ld_sc1(rs, off); o1 = ld_sc1(rs, off + 128); ls = ld_sc1(rs, loff);
        const bool ok = (o0[1] == tag) & (o0[3] == tag) & (o1[1] == tag) & (o1[3] == tag) & (ls[1] == tag) & (ls[3] == tag);
        if (__all(ok)) break;
      }
      TS(6);
      const float l0 = __uint_as_float(ls[0]), l1 = __uint_as_float(ls[2]);
      float M = fmaxf(l0, l1); M = fmaxf(M, dpp<ROR8>(M)); M = xrow_max(M);
      const float e0 = __builtin_amdgcn_exp2f(l0 - M), e1 = __builtin_amdgcn_exp2f(l1 - M);
      float a[5] = {e0 * bflo(o0[0]) + e1 * bflo(o1[0]), e0 * bfhi(o0[0]) + e1 * bfhi(o1[0]), e0 * bflo(o0[2]) + e1 * bflo(o1[2]), e0 * bfhi(o0[2]) + e1 * bfhi(o1[2]), e0 + e1};
#pragma unroll
      for (int r = 0; r < 5; r++) { a[r] += dpp<ROR8>(a[r]); a[r] = xrow_sum(a[r]); }
      const float ri = __builtin_amdgcn_rcpf(a[4]);
      if (lane < 8) *(LAS v2u*)(smem + L_OB + w * 64 + lane * 8) = v2u{pk2(a[0] * ri, a[1] * ri), pk2(a[2] * ri, a[3] * ri)};
      lds_signal(smem, F_O, w, lane);
    }
    TS(7);
  } else {
    const int w = wave - 8;
    if (w == 0 && lane < 34) *(LAS unsigned*)(smem + L_FLAG + lane * 4) = 0u;
    asm volatile("s_waitcnt lgkmcnt(0)" ::: "memory");
    __builtin_amdgcn_s_barrier();
    const int rb = wg >> 6, rcol = (wg & 63) * 16 + w * 4;
    const v2u xraw = *(const GAS v2u*)((const GAS unsigned char*)x_ + rb * 2048 + rcol * 2);
    if (w == 0) {
      const unsigned epoch = __builtin_amdgcn_raw_buffer_load_b32(rs, WS_EPOCH, 0, AUX_SC1);
      if (lane == 0) *(LAS v2u*)(smem + L_TAG) = v2u{epoch + 1u, 1u};
    }
    lds_wait4(smem, F_ISS);
    v4u FD[8], FO[8];
    {
      const __amdgpu_buffer_rsrc_t rd = __builtin_amdgcn_make_buffer_rsrc((void*)wd_, 0, has_wd ? FF * DM * 2 : 0, 0x00020000);
      const __amdgpu_buffer_rsrc_t ro = __builtin_amdgcn_make_buffer_rsrc((void*)wo_, 0, NQ * DM * 2, 0x00020000);
      const int cofs = half * 1024 + w * 256 + c16 * 16;
      const int offd = (32 * v2 + 8 * kq) * (DM * 2) + cofs;
      const int offo = (128 * h2 + 32 * u2 + 2 * kq) * (DM * 2) + cofs;
      v4u R[16];
#pragma unroll
      for (int i = 0; i < 16 + W2THR; i++) {
        if (i < 8) R[i] = __builtin_amdgcn_raw_buffer_load_b128(rd, offd + i * (DM * 2), 0, USE_NT ? 2 : 0);
        else if (i < 16) R[i] = __builtin_amdgcn_raw_buffer_load_b128(ro, offo + (8 * (i & 3) + ((i >> 2) & 1)) * (DM * 2), 0, USE_NT ? 2 : 0);
        SB();
        const int k = i - W2THR;
        if (k >= 0 && (k & 1)) {
          if (k < 8) { tpair(R[k - 1], R[k], FD, k >> 1); }
          else { tpair(R[k - 1], R[k], FO, (k - 8) >> 1); }
          if (k < 8) asm volatile("" :: "v"(FD[0][(k >> 1) & 3]), "v"(FD[7][(k >> 1) & 3])); else asm volatile("" :: "v"(FO[0][((k - 8) >> 1) & 3]), "v"(FO[7][((k - 8) >> 1) & 3]));
          SB();
        }
        if (i == 15) { TS(1); }
      }
    }
    PIN8(FD); PIN8(FO);
    __builtin_amdgcn_s_setprio(3);
    TS(2);
    f32x4 acc[8];
    {
      if (has_wd) lds_wait4(smem, F_H);
      v4u af = *(const LAS v4u*)(smem + L_HB + (c16 >> 2) * 64 + kq * 16);
      if (!has_wd) af = v4u{0u, 0u, 0u, 0u};
#pragma unroll
      for (int j = 0; j < 8; j++) acc[j] = mfma(af, FD[j], z4);
    }
    const unsigned tag = get_tag(smem);
    TS(3);
    {
      lds_wait4(smem, F_O);
      const v4u af = *(const LAS v4u*)(smem + L_OB + (c16 >> 2) * 64 + kq * 16);
#pragma unroll
      for (int j = 0; j < 8; j++) acc[j] = mfma(af, FO[j], acc[j]);
    }
    {
      const int cg = half * 32 + w * 8 + (c16 >> 1);
      const int off = WS_OUT + (kq * 64 + cg) * 8192 + (h2 * 4 + u2) * 64 + (c16 & 1) * 32;
      st_sc1(rs, off, v4u{pk2(acc[0][0], acc[1][0]), tag, pk2(acc[2][0], acc[3][0]), tag});
      st_sc1(rs, off + 16, v4u{pk2(acc[4][0], acc[5][0]), tag, pk2(acc[6][0], acc[7][0]), tag});
    }
    TS(4);
    {
      int off = WS_OUT + wg * 8192 + lane * 64 + w * 16;
      v4u p0, p1;
#pragma unroll 1
      for (unsigned it = 0; it < GSPIN; it++) {
        OPAQUE(off);
        p0 = ld_sc1(rs, off); p1 = ld_sc1(rs, off + 4096);
        const bool ok = (p0[1] == tag) & (p0[3] == tag) & (p1[1] == tag) & (p1[3] == tag);
        if (__all(ok)) break;
        __builtin_amdgcn_s_sleep(1);
      }
      TS(5);
      float a[4] = {bflo(p0[0]) + bflo(p1[0]), bfhi(p0[0]) + bfhi(p1[0]), bflo(p0[2]) + bflo(p1[2]), bfhi(p0[2]) + bfhi(p1[2])};
#pragma unroll
      for (int r = 0; r < 4; r++) a[r] = wave_sum(a[r]);
      if (lane == 0) {
        const v2u o = {pk2(a[0] + bflo(xraw[0]), a[1] + bfhi(xraw[0])), pk2(a[2] + bflo(xraw[1]), a[3] + bfhi(xraw[1]))};
        *(GAS v2u*)((GAS unsigned char*)out_ + rb * 2048 + rcol * 2) = o;
        if (wg == 0 && w == 0) *(GAS unsigned*)(ws + WS_EPOCH) = tag;
      }
    }
    TS(6);
  }
  TS_OUT();
}

extern "C" void kernel_launch(void* const* d_in, const int* in_sizes, int n_in, void* d_out, int out_size, void* d_ws, size_t ws_size,
                              hipStream_t stream) {
  fused_decode<<<NWG, NTHR, 0, stream>>>((const u16*)d_in[0], (const u16*)d_in[1], (const u16*)d_in[2], (const u16*)d_in[3], (const u16*)d_in[4],
                                         (const u16*)d_in[6], (const u16*)d_in[7], (const u16*)d_in[8], (u16*)d_out, (unsigned char*)d_ws);
}
```

```cpp
#define ORDER 2
#define TG 1
#define TNT 64
#define TLDS 256
#define PWG 0
#include <hip/hip_runtime.h>
#include <stdint.h>

#define USE_NT 1
#define STAMP 1
#define NOPOLL 0
#define W2THR 4

#define GAS __attribute__((address_space(1)))
#define LAS __attribute__((address_space(3)))
typedef unsigned v4u __attribute__((ext_vector_type(4)));
typedef unsigned v2u __attribute__((ext_vector_type(2)));
typedef float f32x4 __attribute__((ext_vector_type(4)));
typedef __bf16 bf16x8 __attribute__((ext_vector_type(8)));
typedef unsigned short u16;

constexpr int DM = 1024, NQ = 4096, SK = 8192, DH = 128, WIN = 2048, FF = 3584;
constexpr int NWG = 256, NTHR = 768;

constexpr int WS_EPOCH = 0;
constexpr int WS_QP = 4096;
constexpr int WS_GU = WS_QP + 524288;
constexpr int WS_OP = WS_GU + 917504;
constexpr int WS_LS = WS_OP + 1048576;
constexpr int WS_OUT = WS_LS + 16384;
constexpr int WS_END = WS_OUT + 2097152;
constexpr int WS_STAMP = 8 << 20;

constexpr int L_FLAG = 0;
constexpr int L_TAG = 128;
constexpr int L_XN = 256;
constexpr int L_RED = L_XN + 8192;
constexpr int L_KST = L_RED + 24576;
constexpr int L_QB = L_KST + 32768;
constexpr int L_AO = L_QB + 2304;
constexpr int L_ML = L_AO + 4 * 8 * 528;
constexpr int L_OB = L_ML + 256;
constexpr int L_HB = L_OB + 256;
constexpr int L_END = L_HB + 256;
static_assert(L_END <= 160 * 1024 && L_END > 80 * 1024, "lds");
enum { F_XN = 0, F_Q, F_GU, F_QB, F_AT, F_O, F_H, F_ISS };

constexpr int AUX_SC1 = 16;
constexpr unsigned GSPIN = NOPOLL ? 1u : (1u << 14), LSPIN = 1u << 18;

__device__ __forceinline__ v4u ldw(const GAS void* p) {
  return __builtin_nontemporal_load((const GAS v4u*)p);
}
__device__ __forceinline__ float bflo(unsigned v) { return __uint_as_float(v << 16); }
__device__ __forceinline__ float bfhi(unsigned v) { return __uint_as_float(v & 0xffff0000u); }
__device__ __forceinline__ unsigned pk2(float lo, float hi) {
  typedef __bf16 bf2 __attribute__((ext_vector_type(2)));
  bf2 r; r.x = (__bf16)lo; r.y = (__bf16)hi;
  return __builtin_bit_cast(unsigned, r);
}
__device__ __forceinline__ float rbf(float f) { return (float)(__bf16)f; }
template <int CTRL> __device__ __forceinline__ float dpp(float x) {
  return __builtin_bit_cast(float, __builtin_amdgcn_mov_dpp(__builtin_bit_cast(int, x), CTRL, 0xf, 0xf, true));
}
constexpr int XOR1 = 0xB1, XOR2 = 0x4E, HMIR = 0x141, ROR8 = 0x128, ROR4 = 0x124;
__device__ __forceinline__ float xrow_sum(float x) {
  auto s = __builtin_amdgcn_permlane16_swap(__float_as_uint(x), __float_as_uint(x), false, false);
  x = __uint_as_float(s[0]) + __uint_as_float(s[1]);
  auto t = __builtin_amdgcn_permlane32_swap(__float_as_uint(x), __float_as_uint(x), false, false);
  return __uint_as_float(t[0]) + __uint_as_float(t[1]);
}
__device__ __forceinline__ float xrow_max(float x) {
  auto s = __builtin_amdgcn_permlane16_swap(__float_as_uint(x), __float_as_uint(x), false, false);
  x = fmaxf(__uint_as_float(s[0]), __uint_as_float(s[1]));
  auto t = __builtin_amdgcn_permlane32_swap(__float_as_uint(x), __float_as_uint(x), false, false);
  return fmaxf(__uint_as_float(t[0]), __uint_as_float(t[1]));
}
__device__ __forceinline__ float wave_sum(float x) {
  x += dpp<XOR1>(x); x += dpp<XOR2>(x); x += dpp<HMIR>(x); x += dpp<ROR8>(x);
  return xrow_sum(x);
}
__device__ __forceinline__ f32x4 mfma(v4u a, v4u b, f32x4 c) {
  return __builtin_amdgcn_mfma_f32_16x16x32_bf16(__builtin_bit_cast(bf16x8, a), __builtin_bit_cast(bf16x8, b), c, 0, 0, 0);
}
__device__ __forceinline__ void tpair(const v4u& r0, const v4u& r1, v4u (&F)[8], int m) {
#pragma unroll
  for (int d = 0; d < 4; d++) {
    F[2 * d][m] = __builtin_amdgcn_perm(r1[d], r0[d], 0x05040100u);
    F[2 * d + 1][m] = __builtin_amdgcn_perm(r1[d], r0[d], 0x07060302u);
  }
}
__device__ __forceinline__ void transpose8(const v4u (&R)[8], v4u (&F)[8]) {
#pragma unroll
  for (int m = 0; m < 4; m++) tpair(R[2 * m], R[2 * m + 1], F, m);
}
#define SB() __builtin_amdgcn_sched_barrier(0)
#define PIN8(F) asm volatile("" : "+v"(F[0]), "+v"(F[1]), "+v"(F[2]), "+v"(F[3]), "+v"(F[4]), "+v"(F[5]), "+v"(F[6]), "+v"(F[7]))
#define OPAQUE(x) asm volatile("" : "+v"(x) :: "memory")
__device__ __forceinline__ void lds_signal(LAS unsigned char* smem, int f, int w, int lane) {
  asm volatile("s_waitcnt lgkmcnt(0)" ::: "memory");
  if (lane == 0) *(volatile LAS unsigned*)(smem + L_FLAG + f * 16 + w * 4) = 1u;
}
__device__ __forceinline__ void lds_wait4(LAS unsigned char* smem, int f) {
#pragma unroll 1
  for (unsigned i = 0; i < LSPIN; i++) {
    const v4u v = *(volatile LAS v4u*)(smem + L_FLAG + f * 16);
    if (__builtin_amdgcn_readfirstlane(v[0] & v[1] & v[2] & v[3])) break;
  }
  asm volatile("" ::: "memory");
}
__device__ __forceinline__ unsigned get_tag(LAS unsigned char* smem) {
  v2u v;
#pragma unroll 1
  for (unsigned i = 0; i < LSPIN; i++) {
    v = *(volatile LAS v2u*)(smem + L_TAG);
    if (__builtin_amdgcn_readfirstlane(v[1])) break;
  }
  return __builtin_amdgcn_readfirstlane(v[0]);
}
__device__ __forceinline__ v4u ld_sc1(__amdgpu_buffer_rsrc_t rs, int off) {
  return __builtin_amdgcn_raw_buffer_load_b128(rs, off, 0, AUX_SC1);
}
__device__ __forceinline__ void st_sc1(__amdgpu_buffer_rsrc_t rs, int off, v4u v) { __builtin_amdgcn_raw_buffer_store_b128(v, rs, off, 0, AUX_SC1); }

#define TS(i) do { SB(); ts[i] = __builtin_amdgcn_s_memrealtime(); SB(); } while (0)
#define TS_DECL unsigned long long ts[8] = {0, 0, 0, 0, 0, 0, 0, 0}
#define TS_OUT() do { if (wg == 100 && wave == 0 && lane == 0) { *(GAS unsigned long long*)(ws + WS_STAMP) = ts[0]; } } while (0)

__global__ void __launch_bounds__(NTHR, 3)
fused_decode(const u16* __restrict__ x_, const u16* __restrict__ kc_, const u16* __restrict__ vc_, const u16* __restrict__ wq_,
             const u16* __restrict__ wo_, const u16* __restrict__ wg_, const u16* __restrict__ wu_, const u16* __restrict__ wd_,
             u16* __restrict__ out_, unsigned char* ws_) {
  __shared__ __attribute__((aligned(16))) unsigned char smem_[L_END];
  LAS unsigned char* smem = (LAS unsigned char*)smem_;
  GAS unsigned char* ws = (GAS unsigned char*)ws_;
  const int lane = threadIdx.x & 63;
  const int wave = __builtin_amdgcn_readfirstlane(threadIdx.x >> 6);
  const int wg = blockIdx.x, xc = wg & 7, y = wg >> 3;
  const int c16 = lane & 15, kq = lane >> 4;
  const __amdgpu_buffer_rsrc_t rs = __builtin_amdgcn_make_buffer_rsrc((void*)ws_, 0, WS_END, 0x00020000);
  const int half = (y >> 2) & 1, z = (y & 3) | ((y >> 3) << 2);
  const int h2 = xc * 4 + (z >> 2), u2 = z & 3;
  const bool has_wd = (z != 11) && (z != 15);
  const int v2 = xc * 14 + (z > 11 ? z - 1 : z);
  const f32x4 z4 = {0.f, 0.f, 0.f, 0.f};
  TS_DECL;
  TS(0);

  if (wave < 4) {
    const int w = wave;
    __builtin_amdgcn_s_setprio(3);
    const int ks = xc, hq = y;
    const bool has_f1 = y >= 4;
    const int fb = y >= 30 ? y - 30 : y - 2;
    const GAS unsigned char* xp = (const GAS unsigned char*)x_ + w * 2048 + lane * 16;
    v4u x0 = *(const GAS v4u*)xp, x1 = *(const GAS v4u*)(xp + 1024);
    const int krow = 128 * ks + 32 * w + 8 * kq;
    v4u R[8], G[8], U[8];
    {
      const GAS unsigned char* p = (const GAS unsigned char*)wq_ + (size_t)krow * (NQ * 2) + hq * 256 + c16 * 16;
#pragma unroll
      for (int i = 0; i < 8; i++) R[i] = ldw(p + (size_t)i * (NQ * 2));
    }
    SB();
    __builtin_amdgcn_s_barrier();
    SB();
    const __amdgpu_buffer_rsrc_t rg = __builtin_amdgcn_make_buffer_rsrc((void*)wg_, 0, has_f1 ? DM * FF * 2 : 0, 0x00020000);
    const __amdgpu_buffer_rsrc_t ru = __builtin_amdgcn_make_buffer_rsrc((void*)wu_, 0, has_f1 ? DM * FF * 2 : 0, 0x00020000);
    const int fo = krow * (FF * 2) + fb * 256 + c16 * 16;
    {
      float v[16];
#pragma unroll
      for (int i = 0; i < 4; i++) { v[2 * i] = bflo(x0[i]); v[2 * i + 1] = bfhi(x0[i]); v[8 + 2 * i] = bflo(x1[i]); v[9 + 2 * i] = bfhi(x1[i]); }
      float s = 0.f;
#pragma unroll
      for (int i = 0; i < 16; i++) s += v[i];
      const float mean = wave_sum(s) * (1.0f / DM);
      float q = 0.f;
#pragma unroll
      for (int i = 0; i < 16; i++) { const float d = v[i] - mean; q += d * d; }
      const float var = wave_sum(q) * (1.0f / DM);
      const float mb = rbf(mean);
      const float den = rbf(__builtin_sqrtf(rbf(rbf(var) + 1e-5f)));
      const float rinv = 1.0f / den;
      v4u o0, o1;
#pragma unroll
      for (int i = 0; i < 4; i++) {
        o0[i] = pk2(rbf(v[2 * i] - mb) * rinv, rbf(v[2 * i + 1] - mb) * rinv);
        o1[i] = pk2(rbf(v[8 + 2 * i] - mb) * rinv, rbf(v[9 + 2 * i] - mb) * rinv);
      }
      *(LAS v4u*)(smem + L_XN + w * 2048 + lane * 16) = o0;
      *(LAS v4u*)(smem + L_XN + w * 2048 + 1024 + lane * 16) = o1;
    }
    lds_signal(smem, F_XN, w, lane);
    SB();
#pragma unroll
    for (int i = 0; i < 8; i++) G[i] = __builtin_amdgcn_raw_buffer_load_b128(rg, fo + i * (FF * 2), 0, USE_NT ? 2 : 0);
    SB();
    TS(1);
    lds_wait4(smem, F_XN);
    const v4u xa = *(const LAS v4u*)(smem + L_XN + (c16 >> 2) * 2048 + krow * 2);
    auto tile = [&](const v4u (&W)[8], int mat) {
      v4u F[8];
      transpose8(W, F);
      float d[8];
#pragma unroll
      for (int j = 0; j < 8; j++) d[j] = mfma(xa, F[j], z4)[0];
      LAS f32x4* rp = (LAS f32x4*)(smem + L_RED + w * 6144 + mat * 2048 + kq * 512 + c16 * 32);
      rp[0] = f32x4{d[0], d[1], d[2], d[3]};
      rp[1] = f32x4{d[4], d[5], d[6], d[7]};
    };
    tile(R, 0);
    lds_signal(smem, F_Q, w, lane);
    TS(2);
    const unsigned tag = get_tag(smem);
    lds_wait4(smem, F_Q);
    if (lane < 32) {
      f32x4 a = z4;
#pragma unroll
      for (int ww = 0; ww < 4; ww++) a += *(const LAS f32x4*)(smem + L_RED + ww * 6144 + w * 512 + lane * 16);
      const v4u o = {pk2(a[0], a[1]), tag, pk2(a[2], a[3]), tag};
      st_sc1(rs, WS_QP + ((w * 4 + (hq >> 3)) * 8 + ks) * 4096 + (hq & 7) * 512 + lane * 16, o);
    }
    SB();
    TS(3);
#pragma unroll
    for (int i = 0; i < 8; i++) U[i] = __builtin_amdgcn_raw_buffer_load_b128(ru, fo + i * (FF * 2), 0, USE_NT ? 2 : 0);
    SB();
    if (has_f1) {
      tile(G, 1);
      SB();
      tile(U, 2);
      lds_signal(smem, F_GU, w, lane);
      TS(4);
      lds_wait4(smem, F_GU);
      const int mat = lane >> 5, ch = lane & 31;
      f32x4 a = z4;
#pragma unroll
      for (int ww = 0; ww < 4; ww++) a += *(const LAS f32x4*)(smem + L_RED + ww * 6144 + 2048 + mat * 2048 + w * 512 + ch * 16);
      const v4u o = {pk2(a[0], a[1]), tag, pk2(a[2], a[3]), tag};
      st_sc1(rs, WS_GU + (4 * fb + (ch >> 3)) * 8192 + w * 2048 + mat * 1024 + ks * 128 + (ch & 7) * 16, o);
    }
    TS(5);
    if (has_wd) {
      v4u g, u;
      int off = WS_GU + v2 * 8192 + w * 2048 + lane * 16;
#pragma unroll 1
      for (unsigned it = 0; it < GSPIN; it++) {
        __builtin_amdgcn_s_sleep(8);
        OPAQUE(off);
        g = ld_sc1(rs, off); u = ld_sc1(rs, off + 1024);
        const bool ok = (g[1] == tag) & (g[3] == tag) & (u[1] == tag) & (u[3] == tag);
        if (__all(ok)) break;
      }
      float gv[4] = {bflo(g[0]), bfhi(g[0]), bflo(g[2]), bfhi(g[2])}, uv[4] = {bflo(u[0]), bfhi(u[0]), bflo(u[2]), bfhi(u[2])};
      float hv[4];
#pragma unroll
      for (int r = 0; r < 4; r++) {
        float a = gv[r] + dpp<ROR8>(gv[r]); a = xrow_sum(a);
        float b = uv[r] + dpp<ROR8>(uv[r]); b = xrow_sum(b);
        hv[r] = rbf(a) * rbf(b);
      }
      if (lane < 8) *(LAS v2u*)(smem + L_HB + w * 64 + lane * 8) = v2u{pk2(hv[0], hv[1]), pk2(hv[2], hv[3])};
      lds_signal(smem, F_H, w, lane);
    }
    TS(6);
  } else if (wave < 8) {
    const int w = wave - 4;
    __builtin_amdgcn_s_barrier();
    __builtin_amdgcn_s_setprio(2);
    const int pair = xc * 2 + (y >> 4), sp = y & 15;
    const int b = pair >> 2, g = pair & 3;
    const size_t kv0 = ((size_t)pair * SK + (SK - WIN) + 128 * sp + 32 * w) * (DH * 2);
    v4u KR[8], VR[8], Q[8];
    int qoff = WS_QP + pair * 32768 + w * 1024 + lane * 16;
    lds_wait4(smem, F_Q);
#pragma unroll
    for (int i = 0; i < 8; i++) KR[i] = ldw((const GAS unsigned char*)kc_ + kv0 + i * 1024 + lane * 16);
    SB();
#pragma unroll
    for (int i = 0; i < 8; i++) Q[i] = ld_sc1(rs, qoff + i * 4096);
    SB();
#pragma unroll
    for (int i = 0; i < 8; i++) VR[i] = ldw((const GAS unsigned char*)vc_ + kv0 + (16 * (i >> 2) + 4 * kq + (i & 3)) * 256 + c16 * 16);
    SB();
    __builtin_amdgcn_s_setprio(3);
    lds_signal(smem, F_ISS, w, lane);
    TS(1);
    LAS unsigned char* kst = smem + L_KST + w * 8192;
#pragma unroll
    for (int i = 0; i < 8; i++) { const int key = 4 * i + kq; *(LAS v4u*)(kst + key * 256 + ((c16 ^ (key & 15)) * 16)) = KR[i]; }
    SB();
    const unsigned tag = get_tag(smem);
    {
      bool ok = true;
#pragma unroll
      for (int i = 0; i < 8; i++) ok &= (Q[i][1] == tag) & (Q[i][3] == tag);
      if (!__all(ok)) {
#pragma unroll 1
        for (unsigned it = 0; it < GSPIN; it++) {
          OPAQUE(qoff);
#pragma unroll
          for (int i = 0; i < 8; i++) Q[i] = ld_sc1(rs, qoff + i * 4096);
          ok = true;
#pragma unroll
          for (int i = 0; i < 8; i++) ok &= (Q[i][1] == tag) & (Q[i][3] == tag);
          if (__all(ok)) break;
        }
      }
      float a0 = 0.f, a1 = 0.f, a2 = 0.f, a3 = 0.f;
#pragma unroll
      for (int i = 0; i < 8; i++) { a0 += bflo(Q[i][0]); a1 += bfhi(Q[i][0]); a2 += bflo(Q[i][2]); a3 += bfhi(Q[i][2]); }
      *(LAS v2u*)(smem + L_QB + (2 * w + (lane >> 5)) * 272 + (lane & 31) * 8) = v2u{pk2(a0, a1), pk2(a2, a3)};
    }
    lds_signal(smem, F_QB, w, lane);
    TS(2);
    v4u KF[8];
#pragma unroll
    for (int T = 0; T < 2; T++)
#pragma unroll
      for (int t = 0; t < 4; t++) KF[4 * T + t] = *(const LAS v4u*)(kst + (16 * T + c16) * 256 + (((4 * t + kq) ^ c16) * 16));
    PIN8(KF);
    lds_wait4(smem, F_QB);
    f32x4 S0 = z4, S1 = z4;
#pragma unroll
    for (int t = 0; t < 4; t++) {
      const v4u qf = *(const LAS v4u*)(smem + L_QB + (c16 & 7) * 272 + t * 64 + kq * 16);
      S0 = mfma(KF[t], qf, S0);
      S1 = mfma(KF[4 + t], qf, S1);
    }
    constexpr float C2 = 0.08838834764831845f * 1.4426950408889634f;
    float sv[8] = {S0[0] * C2, S0[1] * C2, S0[2] * C2, S0[3] * C2, S1[0] * C2, S1[1] * C2, S1[2] * C2, S1[3] * C2};
    float m = fmaxf(fmaxf(fmaxf(sv[0], sv[1]), fmaxf(sv[2], sv[3])), fmaxf(fmaxf(sv[4], sv[5]), fmaxf(sv[6], sv[7])));
    m = xrow_max(m);
    float l = 0.f;
#pragma unroll
    for (int i = 0; i < 8; i++) { sv[i] = __builtin_amdgcn_exp2f(sv[i] - m); l += sv[i]; }
    l = xrow_sum(l);
    v4u pf = {pk2(sv[0], sv[1]), pk2(sv[2], sv[3]), pk2(sv[4], sv[5]), pk2(sv[6], sv[7])};
    if (lane < 8) *(LAS v2u*)(smem + L_ML + (w * 8 + lane) * 8) = v2u{__float_as_uint(m), __float_as_uint(l)};
    asm volatile("" : "+v"(pf));
    TS(3);
    v4u VF[8];
    transpose8(VR, VF);
    f32x4 O[8];
#pragma unroll
    for (int j = 0; j < 8; j++) O[j] = mfma(VF[j], pf, z4);
    PIN8(O);
    if (c16 < 8) {
#pragma unroll
      for (int j = 0; j < 8; j++) *(LAS f32x4*)(smem + L_AO + (w * 8 + j) * 528 + kq * 128 + c16 * 16) = O[j];
    }
    lds_signal(smem, F_AT, w, lane);
    TS(4);
    lds_wait4(smem, F_AT);
    {
      const int hh = 2 * w + (lane >> 5), q4 = (lane >> 3) & 3, j = lane & 7;
      float mw[4], lw[4];
      f32x4 ow[4];
#pragma unroll
      for (int ww = 0; ww < 4; ww++) {
        const v2u t = *(const LAS v2u*)(smem + L_ML + (ww * 8 + hh) * 8); mw[ww] = __uint_as_float(t[0]); lw[ww] = __uint_as_float(t[1]);
        ow[ww] = *(const LAS f32x4*)(smem + L_AO + (ww * 8 + j) * 528 + q4 * 128 + hh * 16);
      }
      const float M = fmaxf(fmaxf(mw[0], mw[1]), fmaxf(mw[2], mw[3]));
      float L = 0.f; f32x4 a = z4;
#pragma unroll
      for (int ww = 0; ww < 4; ww++) {
        const float e = __builtin_amdgcn_exp2f(mw[ww] - M);
        L += e * lw[ww];
        a += e * ow[ww];
      }
      const float ri = __builtin_amdgcn_rcpf(L);
      const v4u o = {pk2(a[0] * ri, a[1] * ri), tag, pk2(a[2] * ri, a[3] * ri), tag};
      const int h = g * 8 + hh;
      st_sc1(rs, WS_OP + ((h * 4 + q4) * 4 + b) * 2048 + sp * 128 + j * 16, o);
      if ((lane & 31) == 0) {
        const float lse = M + __builtin_amdgcn_logf(L);
        __builtin_amdgcn_raw_buffer_store_b64(v2u{__float_as_uint(lse), tag}, rs, WS_LS + ((h * 4 + b) * 16 + sp) * 8, 0, AUX_SC1);
      }
    }
    TS(5);
    {
      const int sl = lane >> 3, j = lane & 7;
      int off = WS_OP + ((h2 * 4 + u2) * 4 + w) * 2048 + sl * 256 + j * 16;
      const int loff = WS_LS + ((h2 * 4 + w) * 16 + 2 * sl) * 8;
      v4u o0, o1, ls;
#pragma unroll 1
      for (unsigned it = 0; it < GSPIN; it++) {
        __builtin_amdgcn_s_sleep(2);
        OPAQUE(off);
        o0 = ld_sc1(rs, off); o1 = ld_sc1(rs, off + 128); ls = ld_sc1(rs, loff);
        const bool ok = (o0[1] == tag) & (o0[3] == tag) & (o1[1] == tag) & (o1[3] == tag) & (ls[1] == tag) & (ls[3] == tag);
        if (__all(ok)) break;
      }
      TS(6);
      const float l0 = __uint_as_float(ls[0]), l1 = __uint_as_float(ls[2]);
      float M = fmaxf(l0, l1); M = fmaxf(M, dpp<ROR8>(M)); M = xrow_max(M);
      const float e0 = __builtin_amdgcn_exp2f(l0 - M), e1 = __builtin_amdgcn_exp2f(l1 - M);
      float a[5] = {e0 * bflo(o0[0]) + e1 * bflo(o1[0]), e0 * bfhi(o0[0]) + e1 * bfhi(o1[0]), e0 * bflo(o0[2]) + e1 * bflo(o1[2]), e0 * bfhi(o0[2]) + e1 * bfhi(o1[2]), e0 + e1};
#pragma unroll
      for (int r = 0; r < 5; r++) { a[r] += dpp<ROR8>(a[r]); a[r] = xrow_sum(a[r]); }
      const float ri = __builtin_amdgcn_rcpf(a[4]);
      if (lane < 8) *(LAS v2u*)(smem + L_OB + w * 64 + lane * 8) = v2u{pk2(a[0] * ri, a[1] * ri), pk2(a[2] * ri, a[3] * ri)};
      lds_signal(smem, F_O, w, lane);
    }
    TS(7);
  } else {
    const int w = wave - 8;
    if (w == 0 && lane < 34) *(LAS unsigned*)(smem + L_FLAG + lane * 4) = 0u;
    asm volatile("s_waitcnt lgkmcnt(0)" ::: "memory");
    __builtin_amdgcn_s_barrier();
    const int rb = wg >> 6, rcol = (wg & 63) * 16 + w * 4;
    const v2u xraw = *(const GAS v2u*)((const GAS unsigned char*)x_ + rb * 2048 + rcol * 2);
    if (w == 0) {
      const unsigned epoch = __builtin_amdgcn_raw_buffer_load_b32(rs, WS_EPOCH, 0, AUX_SC1);
      if (lane == 0) *(LAS v2u*)(smem + L_TAG) = v2u{epoch + 1u, 1u};
    }
    lds_wait4(smem, F_ISS);
    v4u FD[8], FO[8];
    {
      const __amdgpu_buffer_rsrc_t rd = __builtin_amdgcn_make_buffer_rsrc((void*)wd_, 0, has_wd ? FF * DM * 2 : 0, 0x00020000);
      const __amdgpu_buffer_rsrc_t ro = __builtin_amdgcn_make_buffer_rsrc((void*)wo_, 0, NQ * DM * 2, 0x00020000);
      const int cofs = half * 1024 + w * 256 + c16 * 16;
      const int offd = (32 * v2 + 8 * kq) * (DM * 2) + cofs;
      const int offo = (128 * h2 + 32 * u2 + 2 * kq) * (DM * 2) + cofs;
      v4u R[16];
#pragma unroll
      for (int i = 0; i < 16 + W2THR; i++) {
        if (i < 8) R[i] = __builtin_amdgcn_raw_buffer_load_b128(rd, offd + i * (DM * 2), 0, USE_NT ? 2 : 0);
        else if (i < 16) R[i] = __builtin_amdgcn_raw_buffer_load_b128(ro, offo + (8 * (i & 3) + ((i >> 2) & 1)) * (DM * 2), 0, USE_NT ? 2 : 0);
        SB();
        const int k = i - W2THR;
        if (k >= 0 && (k & 1)) {
          if (k < 8) { tpair(R[k - 1], R[k], FD, k >> 1); }
          else { tpair(R[k - 1], R[k], FO, (k - 8) >> 1); }
          if (k < 8) asm volatile("" :: "v"(FD[0][(k >> 1) & 3]), "v"(FD[7][(k >> 1) & 3])); else asm volatile("" :: "v"(FO[0][((k - 8) >> 1) & 3]), "v"(FO[7][((k - 8) >> 1) & 3]));
          SB();
        }
        if (i == 15) { TS(1); }
      }
    }
    PIN8(FD); PIN8(FO);
    __builtin_amdgcn_s_setprio(3);
    TS(2);
    f32x4 acc[8];
    {
      if (has_wd) lds_wait4(smem, F_H);
      v4u af = *(const LAS v4u*)(smem + L_HB + (c16 >> 2) * 64 + kq * 16);
      if (!has_wd) af = v4u{0u, 0u, 0u, 0u};
#pragma unroll
      for (int j = 0; j < 8; j++) acc[j] = mfma(af, FD[j], z4);
    }
    const unsigned tag = get_tag(smem);
    TS(3);
    {
      lds_wait4(smem, F_O);
      const v4u af = *(const LAS v4u*)(smem + L_OB + (c16 >> 2) * 64 + kq * 16);
#pragma unroll
      for (int j = 0; j < 8; j++) acc[j] = mfma(af, FO[j], acc[j]);
    }
    {
      const int cg = half * 32 + w * 8 + (c16 >> 1);
      const int off = WS_OUT + (kq * 64 + cg) * 8192 + (h2 * 4 + u2) * 64 + (c16 & 1) * 32;
      st_sc1(rs, off, v4u{pk2(acc[0][0], acc[1][0]), tag, pk2(acc[2][0], acc[3][0]), tag});
      st_sc1(rs, off + 16, v4u{pk2(acc[4][0], acc[5][0]), tag, pk2(acc[6][0], acc[7][0]), tag});
    }
    TS(4);
    {
      int off = WS_OUT + wg * 8192 + lane * 64 + w * 16;
      v4u p0, p1;
#pragma unroll 1
      for (unsigned it = 0; it < GSPIN; it++) {
        OPAQUE(off);
        p0 = ld_sc1(rs, off); p1 = ld_sc1(rs, off + 4096);
        const bool ok = (p0[1] == tag) & (p0[3] == tag) & (p1[1] == tag) & (p1[3] == tag);
        if (__all(ok)) break;
        __builtin_amdgcn_s_sleep(1);
      }
      TS(5);
      float a[4] = {bflo(p0[0]) + bflo(p1[0]), bfhi(p0[0]) + bfhi(p1[0]), bflo(p0[2]) + bflo(p1[2]), bfhi(p0[2]) + bfhi(p1[2])};
#pragma unroll
      for (int r = 0; r < 4; r++) a[r] = wave_sum(a[r]);
      if (lane == 0) {
        const v2u o = {pk2(a[0] + bflo(xraw[0]), a[1] + bfhi(xraw[0])), pk2(a[2] + bflo(xraw[1]), a[3] + bfhi(xraw[1]))};
        *(GAS v2u*)((GAS unsigned char*)out_ + rb * 2048 + rcol * 2) = o;
        if (wg == 0 && w == 0) *(GAS unsigned*)(ws + WS_EPOCH) = tag;
      }
    }
    TS(6);
  }
  TS_OUT();
}

template <int NT, int LDS>
__global__ void __launch_bounds__(NT) tiny(unsigned char* ws) {
  __shared__ unsigned pad[LDS / 4];
  unsigned long long t0 = __builtin_amdgcn_s_memrealtime();
  if (blockIdx.x == (unsigned)(TG / 2) && threadIdx.x < 64) {
    const unsigned long long until = *(volatile unsigned long long*)(ws + WS_STAMP) + 6000ull;
    while (__builtin_amdgcn_s_memrealtime() < until) __builtin_amdgcn_s_sleep(2);
  }
  if (t0 == 1234) { pad[threadIdx.x] = 1; ws[0] = pad[5]; }
}
extern "C" void kernel_launch(void* const* d_in, const int* in_sizes, int n_in, void* d_out, int out_size, void* d_ws, size_t ws_size,
                              hipStream_t stream) {
  fused_decode<<<NWG, NTHR, 0, stream>>>((const u16*)d_in[0], (const u16*)d_in[1], (const u16*)d_in[2], (const u16*)d_in[3], (const u16*)d_in[4],
                                         (const u16*)d_in[6], (const u16*)d_in[7], (const u16*)d_in[8], (u16*)d_out, (unsigned char*)d_ws);
  tiny<TNT, TLDS><<<TG, TNT, 0, stream>>>((unsigned char*)d_ws);
}
```

```cpp
#include <hip/hip_runtime.h>
#include <stdint.h>

#define USE_NT 1
#define STAMP 0
#define NOPOLL 0
#define W2GATE 780
#define W2E 0
#define HGATE 650
#define QGATE 500
#define ODELAY 40
#define FDELAY 30
#define W2THR 16
#define NST 16

#define GAS __attribute__((address_space(1)))
#define LAS __attribute__((address_space(3)))
typedef unsigned v4u __attribute__((ext_vector_type(4)));
typedef unsigned v2u __attribute__((ext_vector_type(2)));
typedef float f32x4 __attribute__((ext_vector_type(4)));
typedef __bf16 bf16x8 __attribute__((ext_vector_type(8)));
typedef unsigned short u16;

constexpr int DM = 1024, NQ = 4096, SK = 8192, DH = 128, WIN = 2048, FF = 3584;
constexpr int NWG = 256, NTHR = 1024;

constexpr int WS_EPOCH = 0;
constexpr int WS_QP = 4096;
constexpr int WS_OP = WS_QP + 524288;
constexpr int WS_LS = WS_OP + 1048576;
constexpr int WS_GU = (2 << 20) + 4096;
constexpr int WS_OUT = WS_GU + 458752;
constexpr int WS_END = WS_OUT + 1048576;
constexpr int WS_STAMP = 8 << 20;

constexpr int L_FLAG = 0;
constexpr int L_TAG = 192;
constexpr int L_XN = 256;
constexpr int L_RED = L_XN + 8192;
constexpr int L_KST = L_RED + 12288;
constexpr int L_QB = L_KST + 32768;
constexpr int L_AO = L_QB + 2304;
constexpr int L_ML = L_AO + 4 * 8 * 528;
constexpr int L_OB = L_ML + 256;
constexpr int L_HB = L_OB + 512;
constexpr int L_END = 84 * 1024;
static_assert(L_HB + 512 <= L_END, "lds");
static_assert(L_END <= 160 * 1024 && L_END > 80 * 1024, "lds");
enum { F_XN = 0, F_Q, F_GU, F_QB, F_ML, F_AT, F_O, F_H, F_OPUB, F_WPUB, F_FIN };

constexpr int AUX_SC1 = 16;
constexpr unsigned GSPIN = NOPOLL ? 1u : (1u << 14), LSPIN = 1u << 18;

__device__ __forceinline__ v4u ldw(const GAS void* p) {
  return __builtin_nontemporal_load((const GAS v4u*)p);
}
__device__ __forceinline__ float bflo(unsigned v) { return __uint_as_float(v << 16); }
__device__ __forceinline__ float bfhi(unsigned v) { return __uint_as_float(v & 0xffff0000u); }
__device__ __forceinline__ unsigned pk2(float lo, float hi) {
  typedef __bf16 bf2 __attribute__((ext_vector_type(2)));
  bf2 r; r.x = (__bf16)lo; r.y = (__bf16)hi;
  return __builtin_bit_cast(unsigned, r);
}
__device__ __forceinline__ float rbf(float f) { return (float)(__bf16)f; }
template <int CTRL> __device__ __forceinline__ float dpp(float x) {
  return __builtin_bit_cast(float, __builtin_amdgcn_mov_dpp(__builtin_bit_cast(int, x), CTRL, 0xf, 0xf, true));
}
constexpr int XOR1 = 0xB1, XOR2 = 0x4E, HMIR = 0x141, ROR8 = 0x128, ROR4 = 0x124;
__device__ __forceinline__ float xrow_sum(float x) {
  auto s = __builtin_amdgcn_permlane16_swap(__float_as_uint(x), __float_as_uint(x), false, false);
  x = __uint_as_float(s[0]) + __uint_as_float(s[1]);
  auto t = __builtin_amdgcn_permlane32_swap(__float_as_uint(x), __float_as_uint(x), false, false);
  return __uint_as_float(t[0]) + __uint_as_float(t[1]);
}
__device__ __forceinline__ float xrow_max(float x) {
  auto s = __builtin_amdgcn_permlane16_swap(__float_as_uint(x), __float_as_uint(x), false, false);
  x = fmaxf(__uint_as_float(s[0]), __uint_as_float(s[1]));
  auto t = __builtin_amdgcn_permlane32_swap(__float_as_uint(x), __float_as_uint(x), false, false);
  return fmaxf(__uint_as_float(t[0]), __uint_as_float(t[1]));
}
__device__ __forceinline__ float wave_sum(float x) {
  x += dpp<XOR1>(x); x += dpp<XOR2>(x); x += dpp<HMIR>(x); x += dpp<ROR8>(x);
  return xrow_sum(x);
}
__device__ __forceinline__ f32x4 mfma(v4u a, v4u b, f32x4 c) {
  return __builtin_amdgcn_mfma_f32_16x16x32_bf16(__builtin_bit_cast(bf16x8, a), __builtin_bit_cast(bf16x8, b), c, 0, 0, 0);
}
__device__ __forceinline__ void tpair(const v4u& r0, const v4u& r1, v4u (&F)[8], int m) {
#pragma unroll
  for (int d = 0; d < 4; d++) {
    F[2 * d][m] = __builtin_amdgcn_perm(r1[d], r0[d], 0x05040100u);
    F[2 * d + 1][m] = __builtin_amdgcn_perm(r1[d], r0[d], 0x07060302u);
  }
}
__device__ __forceinline__ void transpose8(const v4u (&R)[8], v4u (&F)[8]) {
#pragma unroll
  for (int m = 0; m < 4; m++) tpair(R[2 * m], R[2 * m + 1], F, m);
}
__device__ __forceinline__ v4u tagpack(float a, float b, float c, float d, unsigned tb) {
  return v4u{(__float_as_uint(a) & 0xffffff00u) | tb, (__float_as_uint(b) & 0xffffff00u) | tb, (__float_as_uint(c) & 0xffffff00u) | tb,
             (__float_as_uint(d) & 0xffffff00u) | tb};
}
__device__ __forceinline__ unsigned tagbad(const v4u& v, unsigned tb) { return ((v[0] ^ tb) | (v[1] ^ tb) | (v[2] ^ tb) | (v[3] ^ tb)) & 0xffu; }
__device__ __forceinline__ float asf(unsigned v) { return __uint_as_float(v); }

#define SB() __builtin_amdgcn_sched_barrier(0)
#define PIN8(F) asm volatile("" : "+v"(F[0]), "+v"(F[1]), "+v"(F[2]), "+v"(F[3]), "+v"(F[4]), "+v"(F[5]), "+v"(F[6]), "+v"(F[7]))
#define OPAQUE(x) asm volatile("" : "+v"(x) :: "memory")
__device__ __forceinline__ void lds_signal(LAS unsigned char* smem, int f, int w, int lane) {
  asm volatile("s_waitcnt lgkmcnt(0)" ::: "memory");
  if (lane == 0) *(volatile LAS unsigned*)(smem + L_FLAG + f * 16 + w * 4) = 1u;
}
template <int SLEEP = 0>
__device__ __forceinline__ void lds_wait4(LAS unsigned char* smem, int f) {
#pragma unroll 1
  for (unsigned i = 0; i < LSPIN; i++) {
    const v4u v = *(volatile LAS v4u*)(smem + L_FLAG + f * 16);
    if (__builtin_amdgcn_readfirstlane(v[0] & v[1] & v[2] & v[3])) break;
    if (SLEEP) __builtin_amdgcn_s_sleep(SLEEP);
  }
  asm volatile("" ::: "memory");
}
__device__ __forceinline__ unsigned get_tag(LAS unsigned char* smem) {
  v2u v;
#pragma unroll 1
  for (unsigned i = 0; i < LSPIN; i++) {
    v = *(volatile LAS v2u*)(smem + L_TAG);
    if (__builtin_amdgcn_readfirstlane(v[1])) break;
    __builtin_amdgcn_s_sleep(2);
  }
  return __builtin_amdgcn_readfirstlane(v[0]);
}
__device__ __forceinline__ void delay_ticks(unsigned n) {
  if (n == 0) return;
  const unsigned long long t = __builtin_amdgcn_s_memrealtime() + n;
  while (__builtin_amdgcn_s_memrealtime() < t) __builtin_amdgcn_s_sleep(1);
}
__device__ __forceinline__ v4u ld_sc1(__amdgpu_buffer_rsrc_t rs, int off) {
  return __builtin_amdgcn_raw_buffer_load_b128(rs, off, 0, AUX_SC1);
}
__device__ __forceinline__ void st_sc1(__amdgpu_buffer_rsrc_t rs, int off, v4u v) { __builtin_amdgcn_raw_buffer_store_b128(v, rs, off, 0, AUX_SC1); }

#define TS(i)
#define TS_DECL
#define TS_OUT()

__global__ void __launch_bounds__(NTHR, 4)
fused_decode(const u16* __restrict__ x_, const u16* __restrict__ kc_, const u16* __restrict__ vc_, const u16* __restrict__ wq_,
             const u16* __restrict__ wo_, const u16* __restrict__ wg_, const u16* __restrict__ wu_, const u16* __restrict__ wd_,
             u16* __restrict__ out_, unsigned char* ws_) {
  __shared__ __attribute__((aligned(16))) unsigned char smem_[L_END];
  LAS unsigned char* smem = (LAS unsigned char*)smem_;
  GAS unsigned char* ws = (GAS unsigned char*)ws_;
  const int lane = threadIdx.x & 63;
  const int wave = __builtin_amdgcn_readfirstlane(threadIdx.x >> 6);
  const int wg = blockIdx.x, xc = wg & 7, y = wg >> 3;
  const int c16 = lane & 15, kq = lane >> 4, w = wave & 3;
  const __amdgpu_buffer_rsrc_t rs = __builtin_amdgcn_make_buffer_rsrc((void*)ws_, 0, WS_END, 0x00020000);
  const f32x4 z4 = {0.f, 0.f, 0.f, 0.f};
  TS_DECL;
  TS(0);
  const unsigned long long tstart = __builtin_amdgcn_s_memrealtime();
  asm volatile("" :: "s"(x_), "s"(kc_), "s"(vc_), "s"(wq_), "s"(wo_), "s"(wg_), "s"(wu_), "s"(wd_), "s"(out_), "s"(ws_), "s"(tstart));
  const int ks = xc >> 1;
  const int frow = 256 * ks + 64 * w + 8 * (lane >> 3);
  const int qcb = y * 2 + (xc & 1);
  const int cq = y & 3, z = y >> 2;
  const int h2 = xc * 4 + (z >> 1), u2 = z & 1;
  const bool has_wd = z != 7;
  const int v2 = xc * 7 + z;
  const int pair = xc * 2 + (y >> 4), sp = y & 15;
  auto tile1 = [&](const v4u (&W)[8], const v4u xq, int mat) {
    const bool par = lane & 8;
    float v[8];
#pragma unroll
    for (int d = 0; d < 4; d++) {
      v4u lo, hi;
#pragma unroll
      for (int m = 0; m < 4; m++) {
        lo[m] = __builtin_amdgcn_perm(W[2 * m + 1][d], W[2 * m][d], 0x05040100u);
        hi[m] = __builtin_amdgcn_perm(W[2 * m + 1][d], W[2 * m][d], 0x07060302u);
      }
      const f32x4 a = mfma(xq, lo, z4), b = mfma(xq, hi, z4);
      v[2 * d] = par ? a[1] : a[0]; v[2 * d + 1] = par ? b[1] : b[0];
    }
    float d[4];
#pragma unroll
    for (int j = 0; j < 4; j++) {
      const float keep = par ? v[j + 4] : v[j], send = par ? v[j] : v[j + 4];
      d[j] = keep + dpp<ROR8>(send);
    }
    *(LAS f32x4*)(smem + L_RED + mat * 4096 + w * 1024 + kq * 256 + (lane & 7) * 32 + (lane & 8) * 2) = f32x4{d[0], d[1], d[2], d[3]};
  };
  auto red1 = [&](int mat, unsigned tb) {
    f32x4 a = z4;
#pragma unroll
    for (int ww = 0; ww < 4; ww++) a += *(const LAS f32x4*)(smem + L_RED + mat * 4096 + ww * 1024 + lane * 16);
    return tagpack(a[0], a[1], a[2], a[3], tb);
  };
  const int xqoff = L_XN + (c16 >> 2) * 2048 + (256 * ks + 64 * w + 16 * kq + 8 * (c16 & 1)) * 2;

  if (wave < 4) {
    const int b = pair >> 2, g = pair & 3;
    const size_t kv0 = ((size_t)pair * SK + (SK - WIN) + 128 * sp + 32 * w) * (DH * 2);
    __builtin_amdgcn_s_setprio(3);
    const GAS unsigned char* xp = (const GAS unsigned char*)x_ + w * 2048 + lane * 16;
    const v4u x0 = *(const GAS v4u*)xp, x1 = *(const GAS v4u*)(xp + 1024);
    const unsigned epoch = __builtin_amdgcn_raw_buffer_load_b32(rs, WS_EPOCH + (w ? 256 : 0), 0, AUX_SC1);
    SB();
    TS(1);
    __builtin_amdgcn_s_barrier();
    SB();
    {
      float v[16];
#pragma unroll
      for (int i = 0; i < 4; i++) { v[2 * i] = bflo(x0[i]); v[2 * i + 1] = bfhi(x0[i]); v[8 + 2 * i] = bflo(x1[i]); v[9 + 2 * i] = bfhi(x1[i]); }
      const float piv = asf(__builtin_amdgcn_readfirstlane(__float_as_uint(v[0])));
      float s = 0.f, q = 0.f;
#pragma unroll
      for (int i = 0; i < 16; i++) { const float d = v[i] - piv; s += d; q += d * d; }
      s = wave_sum(s) * (1.0f / DM); q = wave_sum(q) * (1.0f / DM);
      const float var = fmaxf(q - s * s, 0.f);
      const float mb = rbf(piv + s);
      const float den = rbf(__builtin_amdgcn_sqrtf(rbf(rbf(var) + 1e-5f)));
      const float rinv = __builtin_amdgcn_rcpf(den);
      v4u o0, o1;
#pragma unroll
      for (int i = 0; i < 4; i++) {
        o0[i] = pk2(rbf(v[2 * i] - mb) * rinv, rbf(v[2 * i + 1] - mb) * rinv);
        o1[i] = pk2(rbf(v[8 + 2 * i] - mb) * rinv, rbf(v[9 + 2 * i] - mb) * rinv);
      }
      *(LAS v4u*)(smem + L_XN + w * 2048 + lane * 16) = o0;
      *(LAS v4u*)(smem + L_XN + w * 2048 + 1024 + lane * 16) = o1;
    }
    lds_signal(smem, F_XN, w, lane);
    if (w == 0 && lane == 0) *(LAS v2u*)(smem + L_TAG) = v2u{epoch + 1u, 1u};
    asm volatile("" :: "v"(epoch));
    TS(8);
    __builtin_amdgcn_s_setprio(1);
    v4u KR[8], VR[8];
#pragma unroll
    for (int i = 0; i < 8; i++) KR[i] = ldw((const GAS unsigned char*)kc_ + kv0 + i * 1024 + lane * 16);
    SB();
#pragma unroll
    for (int i = 0; i < 8; i++) VR[i] = ldw((const GAS unsigned char*)vc_ + kv0 + (16 * (i >> 2) + 4 * kq + (i & 3)) * 256 + c16 * 16);
    SB();
    __builtin_amdgcn_s_setprio(3);
    TS(9);
    LAS unsigned char* kst = smem + L_KST + w * 8192;
#pragma unroll
    for (int i = 0; i < 8; i++) { const int key = 4 * i + kq; *(LAS v4u*)(kst + key * 256 + ((c16 ^ (key & 15)) * 16)) = KR[i]; }
    SB();
    const unsigned tag = get_tag(smem), tb = tag & 0xffu;
    TS(2);
    v4u KF[8];
#pragma unroll
    for (int T = 0; T < 2; T++)
#pragma unroll
      for (int t = 0; t < 4; t++) KF[4 * T + t] = *(const LAS v4u*)(kst + (16 * T + c16) * 256 + (((4 * t + kq) ^ c16) * 16));
    PIN8(KF);
    lds_wait4<1>(smem, F_QB);
    TS(13);
    f32x4 S0 = z4, S1 = z4;
#pragma unroll
    for (int t = 0; t < 4; t++) {
      const v4u qf = *(const LAS v4u*)(smem + L_QB + (c16 & 7) * 272 + t * 64 + kq * 16);
      S0 = mfma(KF[t], qf, S0);
      S1 = mfma(KF[4 + t], qf, S1);
    }
    constexpr float SC = 0.08838834764831845f, L2E = 1.4426950408889634f;
    float sv[8] = {S0[0], S0[1], S0[2], S0[3], S1[0], S1[1], S1[2], S1[3]};
#pragma unroll
    for (int i = 0; i < 8; i++) sv[i] = rbf(rbf(sv[i]) * SC) * L2E;
    float m = fmaxf(fmaxf(fmaxf(sv[0], sv[1]), fmaxf(sv[2], sv[3])), fmaxf(fmaxf(sv[4], sv[5]), fmaxf(sv[6], sv[7])));
    m = xrow_max(m);
    float l = 0.f;
#pragma unroll
    for (int i = 0; i < 8; i++) { sv[i] = __builtin_amdgcn_exp2f(sv[i] - m); l += sv[i]; }
    l = xrow_sum(l);
    if (lane < 8) *(LAS v2u*)(smem + L_ML + (w * 8 + lane) * 8) = v2u{__float_as_uint(m), __float_as_uint(l)};
    lds_signal(smem, F_ML, w, lane);
    lds_wait4(smem, F_ML);
    v4u pf;
    float lse;
    {
      float mw[4], lw[4];
#pragma unroll
      for (int ww = 0; ww < 4; ww++) { const v2u t = *(const LAS v2u*)(smem + L_ML + (ww * 8 + (c16 & 7)) * 8); mw[ww] = asf(t[0]); lw[ww] = asf(t[1]); }
      const float M = fmaxf(fmaxf(mw[0], mw[1]), fmaxf(mw[2], mw[3]));
      float L = 0.f;
#pragma unroll
      for (int ww = 0; ww < 4; ww++) L += __builtin_amdgcn_exp2f(mw[ww] - M) * lw[ww];
      const float f = __builtin_amdgcn_exp2f(m - M) * __builtin_amdgcn_rcpf(L);
      pf = v4u{pk2(sv[0] * f, sv[1] * f), pk2(sv[2] * f, sv[3] * f), pk2(sv[4] * f, sv[5] * f), pk2(sv[6] * f, sv[7] * f)};
      lse = M + __builtin_amdgcn_logf(L);
    }
    asm volatile("" : "+v"(pf), "+v"(lse));
    TS(3);
    v4u VF[8];
    transpose8(VR, VF);
    f32x4 O[8];
#pragma unroll
    for (int j = 0; j < 8; j++) O[j] = mfma(VF[j], pf, z4);
    PIN8(O);
    if (c16 < 8) {
#pragma unroll
      for (int j = 0; j < 8; j++) *(LAS f32x4*)(smem + L_AO + (w * 8 + j) * 528 + kq * 128 + c16 * 16) = O[j];
    }
    lds_signal(smem, F_AT, w, lane);
    if (w == 0 && lane < 8)
      __builtin_amdgcn_raw_buffer_store_b64(v2u{__float_as_uint(lse), tag}, rs, WS_LS + (((g * 8 + lane) * 4 + b) * 16 + sp) * 8, 0, AUX_SC1);
    TS(4);
    lds_wait4(smem, F_AT);
    {
      const int hh = 2 * w + (lane >> 5), q4 = (lane >> 3) & 3, j = lane & 7;
      f32x4 a = z4;
#pragma unroll
      for (int ww = 0; ww < 4; ww++) a += *(const LAS f32x4*)(smem + L_AO + (ww * 8 + j) * 528 + q4 * 128 + hh * 16);
      st_sc1(rs, WS_OP + (((g * 8 + hh) * 4 + q4) * 4 + b) * 2048 + sp * 128 + j * 16, tagpack(a[0], a[1], a[2], a[3], tb));
    }
    lds_signal(smem, F_OPUB, w, lane);
    TS(5);
  } else if (wave < 8) {
    __builtin_amdgcn_s_setprio(3);
    v4u R[8];
    {
      const __amdgpu_buffer_rsrc_t rq = __builtin_amdgcn_make_buffer_rsrc((void*)wq_, 0, DM * NQ * 2, 0x00020000);
      const int qo = frow * (NQ * 2) + qcb * 128 + (lane & 7) * 16;
#pragma unroll
      for (int i = 0; i < 8; i++) R[i] = __builtin_amdgcn_raw_buffer_load_b128(rq, qo, i * (NQ * 2), USE_NT ? 2 : 0);
    }
    SB();
    TS(1);
    __builtin_amdgcn_s_barrier();
    SB();
    const int r128 = (wg >> 4) * 8 + (wg & 7), rhf = (wg >> 3) & 1;
    const int oofs = (r128 >> 5) * 2048 + ((r128 & 31) * 32 + rhf * 16 + (lane & 3) * 4) * 2;
    const v2u xraw = *(const GAS v2u*)((const GAS unsigned char*)x_ + oofs);
    {
      lds_wait4<1>(smem, F_XN);
      const v4u xq = *(const LAS v4u*)(smem + xqoff);
      tile1(R, xq, 0);
      TS(2);
    }
    lds_signal(smem, F_Q, w, lane);
    const unsigned tag = get_tag(smem), tb = tag & 0xffu;
    if (w == 3) {
      lds_wait4(smem, F_Q);
      st_sc1(rs, WS_QP + ((kq * 4 + (qcb >> 4)) * 4 + ks) * 4096 + (qcb & 15) * 256 + c16 * 16, red1(0, tb));
    }
    TS(3);
    {
      v4u Q[4];
      int qoff = WS_QP + pair * 16384 + w * 1024 + lane * 16;
      while (__builtin_amdgcn_s_memrealtime() < tstart + QGATE) __builtin_amdgcn_s_sleep(2);
      TS(12);
      unsigned bad = 1;
#pragma unroll 1
      for (unsigned it = 0; it < GSPIN; it++) {
        OPAQUE(qoff);
        if (bad) {
#pragma unroll
          for (int i = 0; i < 4; i++) Q[i] = ld_sc1(rs, qoff + i * 4096);
          bad = 0;
#pragma unroll
          for (int i = 0; i < 4; i++) bad |= tagbad(Q[i], tb);
        }
        if (__all(bad == 0)) break;
      }
      float a0 = 0.f, a1 = 0.f, a2 = 0.f, a3 = 0.f;
#pragma unroll
      for (int i = 0; i < 4; i++) { a0 += asf(Q[i][0]); a1 += asf(Q[i][1]); a2 += asf(Q[i][2]); a3 += asf(Q[i][3]); }
      *(LAS v2u*)(smem + L_QB + (2 * w + (lane >> 5)) * 272 + (lane & 31) * 8) = v2u{pk2(a0, a1), pk2(a2, a3)};
      lds_signal(smem, F_QB, w, lane);
    }
    TS(5);
    {
      const int sl = lane >> 3, j = lane & 7;
      int off = WS_OP + ((h2 * 4 + 2 * u2) * 4 + w) * 2048 + sl * 256 + j * 16;
      const int loff = WS_LS + ((h2 * 4 + w) * 16 + 2 * sl) * 8;
      v4u o[4], ls;
      lds_wait4<1>(smem, F_OPUB);
      TS(8);
      delay_ticks(ODELAY);
      unsigned bad = 1;
#pragma unroll 1
      for (unsigned it = 0; it < GSPIN; it++) {
        OPAQUE(off);
        if (bad) {
          ls = ld_sc1(rs, loff);
          o[0] = ld_sc1(rs, off); o[1] = ld_sc1(rs, off + 128); o[2] = ld_sc1(rs, off + 8192); o[3] = ld_sc1(rs, off + 8320);
          bad = tagbad(o[0], tb) | tagbad(o[1], tb) | tagbad(o[2], tb) | tagbad(o[3], tb) | (ls[1] ^ tag) | (ls[3] ^ tag);
        }
        if (__all(bad == 0)) break;
        __builtin_amdgcn_s_sleep(1);
      }
      TS(9);
      const float l0 = asf(ls[0]), l1 = asf(ls[2]);
      float M = fmaxf(l0, l1); M = fmaxf(M, dpp<ROR8>(M)); M = xrow_max(M);
      float e0 = __builtin_amdgcn_exp2f(l0 - M), e1 = __builtin_amdgcn_exp2f(l1 - M);
      float den = e0 + e1; den += dpp<ROR8>(den); den = xrow_sum(den);
      const float ri = __builtin_amdgcn_rcpf(den);
      e0 *= ri; e1 *= ri;
      float a[8];
#pragma unroll
      for (int r = 0; r < 4; r++) { a[r] = e0 * asf(o[0][r]) + e1 * asf(o[1][r]); a[4 + r] = e0 * asf(o[2][r]) + e1 * asf(o[3][r]); }
#pragma unroll
      for (int r = 0; r < 8; r++) { a[r] += dpp<ROR8>(a[r]); a[r] = xrow_sum(a[r]); }
      if (lane < 8) {
        *(LAS v2u*)(smem + L_OB + w * 128 + lane * 8) = v2u{pk2(a[0], a[1]), pk2(a[2], a[3])};
        *(LAS v2u*)(smem + L_OB + w * 128 + 64 + lane * 8) = v2u{pk2(a[4], a[5]), pk2(a[6], a[7])};
      }
      lds_signal(smem, F_O, w, lane);
    }
    TS(7);
    if (w == 0) {
      int off = WS_OUT + r128 * 8192 + (lane >> 2) * 128 + rhf * 64 + (lane & 3) * 16;
      v4u p[4];
      lds_wait4<1>(smem, F_WPUB);
      TS(10);
      delay_ticks(FDELAY);
      unsigned bad = 1;
#pragma unroll 1
      for (unsigned it = 0; it < GSPIN; it++) {
        OPAQUE(off);
        if (bad) {
#pragma unroll
          for (int i = 0; i < 4; i++) p[i] = ld_sc1(rs, off + i * 2048);
          bad = tagbad(p[0], tb) | tagbad(p[1], tb) | tagbad(p[2], tb) | tagbad(p[3], tb);
        }
        if (__all(bad == 0)) break;
        __builtin_amdgcn_s_sleep(1);
      }
      TS(11);
      float a[4];
#pragma unroll
      for (int r = 0; r < 4; r++) {
        a[r] = (asf(p[0][r]) + asf(p[1][r])) + (asf(p[2][r]) + asf(p[3][r]));
        a[r] += dpp<ROR4>(a[r]); a[r] += dpp<ROR8>(a[r]); a[r] = xrow_sum(a[r]);
      }
      if (lane < 4) {
        *(GAS v2u*)((GAS unsigned char*)out_ + oofs) = v2u{pk2(a[0] + bflo(xraw[0]), a[1] + bfhi(xraw[0])), pk2(a[2] + bflo(xraw[1]), a[3] + bfhi(xraw[1]))};
        if (wg == 0 && lane == 0) *(GAS unsigned*)(ws + WS_EPOCH) = tag;
      }
    }
    TS(6);
  } else if (wave < 12) {
    __builtin_amdgcn_s_barrier();
    const bool has_f1 = y >= 4;
    const int cbg = (y >= 31 ? y - 31 : y - 3) * 2 + (xc & 1), cbu = (y >= 30 ? y - 30 : y - 2) * 2 + (xc & 1);
    unsigned tb;
    if (has_f1) {
      const __amdgpu_buffer_rsrc_t rg = __builtin_amdgcn_make_buffer_rsrc((void*)wg_, 0, DM * FF * 2, 0x00020000);
      const __amdgpu_buffer_rsrc_t ru = __builtin_amdgcn_make_buffer_rsrc((void*)wu_, 0, DM * FF * 2, 0x00020000);
      const int fo = frow * (FF * 2) + (lane & 7) * 16;
      v4u G[8], U[8];
      __builtin_amdgcn_s_setprio(2);
#pragma unroll
      for (int i = 0; i < 8; i++) G[i] = __builtin_amdgcn_raw_buffer_load_b128(rg, fo + cbg * 128, i * (FF * 2), USE_NT ? 2 : 0);
      SB();
#pragma unroll
      for (int i = 0; i < 8; i++) U[i] = __builtin_amdgcn_raw_buffer_load_b128(ru, fo + cbu * 128, i * (FF * 2), USE_NT ? 2 : 0);
      SB();
      __builtin_amdgcn_s_setprio(3);
      TS(9);
      lds_wait4<1>(smem, F_XN);
      const v4u xq = *(const LAS v4u*)(smem + xqoff);
      tb = get_tag(smem) & 0xffu;
      tile1(G, xq, 1);
      SB();
      TS(10);
      tile1(U, xq, 2);
      lds_signal(smem, F_GU, w, lane);
      TS(4);
      if (w < 2) {
        lds_wait4(smem, F_GU);
        st_sc1(rs, WS_GU + (w ? cbu : cbg) * 8192 + kq * 2048 + w * 1024 + ks * 256 + c16 * 16, red1(1 + w, tb));
      }
      TS(14);
    } else {
      tb = get_tag(smem) & 0xffu;
    }
    if (has_wd && w >= 2) {
      __builtin_amdgcn_s_setprio(3);
      v4u g[2], u[2];
      int off = WS_GU + v2 * 8192 + (w - 2) * 4096 + lane * 16;
      while (__builtin_amdgcn_s_memrealtime() < tstart + HGATE) __builtin_amdgcn_s_sleep(2);
      TS(12);
      unsigned bad = 1;
#pragma unroll 1
      for (unsigned it = 0; it < GSPIN; it++) {
        OPAQUE(off);
        if (bad) {
          g[0] = ld_sc1(rs, off); u[0] = ld_sc1(rs, off + 1024); g[1] = ld_sc1(rs, off + 2048); u[1] = ld_sc1(rs, off + 3072);
          bad = tagbad(g[0], tb) | tagbad(u[0], tb) | tagbad(g[1], tb) | tagbad(u[1], tb);
        }
        if (__all(bad == 0)) break;
        __builtin_amdgcn_s_sleep(4);
      }
#pragma unroll
      for (int t = 0; t < 2; t++) {
        float hv[4];
#pragma unroll
        for (int r = 0; r < 4; r++) hv[r] = rbf(xrow_sum(asf(g[t][r]))) * rbf(xrow_sum(asf(u[t][r])));
        if (lane < 16) *(LAS v2u*)(smem + L_HB + (2 * (w - 2) + t) * 128 + lane * 8) = v2u{pk2(hv[0], hv[1]), pk2(hv[2], hv[3])};
      }
      lds_signal(smem, F_H, w, lane);
      lds_signal(smem, F_H, w - 2, lane);
      TS(13);
    }
  } else {
    if (w == 0 && lane < 50) *(LAS unsigned*)(smem + L_FLAG + lane * 4) = 0u;
    asm volatile("s_waitcnt lgkmcnt(0)" ::: "memory");
    __builtin_amdgcn_s_barrier();
    const __amdgpu_buffer_rsrc_t rd = __builtin_amdgcn_make_buffer_rsrc((void*)wd_, 0, has_wd ? FF * DM * 2 : 0, 0x00020000);
    const __amdgpu_buffer_rsrc_t ro = __builtin_amdgcn_make_buffer_rsrc((void*)wo_, 0, NQ * DM * 2, 0x00020000);
    const int r8 = lane >> 3;
    const int cofs = cq * 512 + w * 128 + (lane & 7) * 16;
    const int offd = (64 * v2 + 8 * r8) * (DM * 2) + cofs;
    const int offo = (128 * h2 + 64 * u2 + 32 * (r8 >> 2) + 2 * (r8 & 3)) * (DM * 2) + cofs;
    v4u FD[8], FO[8];
    {
      v4u W[16];
      __builtin_amdgcn_s_setprio(2);
#pragma unroll
      for (int i = 0; i < 16; i++) {
        if (i == W2E) { SB(); __builtin_amdgcn_s_setprio(0); TS(9); if (W2GATE) { while (__builtin_amdgcn_s_memrealtime() < tstart + W2GATE) __builtin_amdgcn_s_sleep(2); } SB(); }
        if (i < 8) W[i] = __builtin_amdgcn_raw_buffer_load_b128(ro, offo, (8 * (i & 3) + ((i >> 2) & 1)) * (DM * 2), USE_NT ? 2 : 0);
        else W[i] = __builtin_amdgcn_raw_buffer_load_b128(rd, offd, (i - 8) * (DM * 2), USE_NT ? 2 : 0);
      }
      SB();
      TS(2);
#pragma unroll
      for (int m = 0; m < 4; m++) tpair(W[2 * m], W[2 * m + 1], FO, m);
      PIN8(FO);
#pragma unroll
      for (int m = 0; m < 4; m++) tpair(W[8 + 2 * m], W[9 + 2 * m], FD, m);
    }
    PIN8(FD);
    __builtin_amdgcn_s_setprio(3);
    TS(3);
    f32x4 acc[8];
    const int aoff = (c16 >> 2) * 128 + kq * 32 + (c16 & 1) * 16;
    {
      if (has_wd) lds_wait4<1>(smem, F_H);
      v4u af = *(const LAS v4u*)(smem + L_HB + aoff);
      if (!has_wd) af = v4u{0u, 0u, 0u, 0u};
#pragma unroll
      for (int j = 0; j < 8; j++) acc[j] = mfma(af, FD[j], z4);
    }
    const unsigned tb = get_tag(smem) & 0xffu;
    {
      TS(7);
      lds_wait4<1>(smem, F_O);
      TS(8);
      const v4u af = *(const LAS v4u*)(smem + L_OB + aoff);
#pragma unroll
      for (int j = 0; j < 8; j++) acc[j] = mfma(af, FO[j], acc[j]);
    }
    {
      const bool par = lane & 8;
      float d[4];
#pragma unroll
      for (int j = 0; j < 4; j++) {
        const float keep = par ? acc[j + 4][1] : acc[j][0], send = par ? acc[j][1] : acc[j + 4][0];
        d[j] = keep + dpp<ROR8>(send);
      }
      const int off = WS_OUT + (kq * 32 + cq * 8 + w * 2 + ((lane & 7) >> 2)) * 8192 + (h2 * 2 + u2) * 128 + (lane & 3) * 32 + (lane & 8) * 2;
      st_sc1(rs, off, tagpack(d[0], d[1], d[2], d[3], tb));
    }
    lds_signal(smem, F_WPUB, w, lane);
    TS(4);
  }
  TS_OUT();
}

extern "C" void kernel_launch(void* const* d_in, const int* in_sizes, int n_in, void* d_out, int out_size, void* d_ws, size_t ws_size,
                              hipStream_t stream) {
  fused_decode<<<NWG, NTHR, 0, stream>>>((const u16*)d_in[0], (const u16*)d_in[1], (const u16*)d_in[2], (const u16*)d_in[3], (const u16*)d_in[4],
                                         (const u16*)d_in[6], (const u16*)d_in[7], (const u16*)d_in[8], (u16*)d_out, (unsigned char*)d_ws);
}
```

```cpp
#include <hip/hip_runtime.h>
#include <stdint.h>

#define QGATE 500
#define W2GATE 650
#define HGATE 710
#define OGATE 965
#define ODELAY 40
#define FDELAY 30
#define TSTEP 90
#define BALANCE 1

#define GAS __attribute__((address_space(1)))
#define LAS __attribute__((address_space(3)))
typedef unsigned v4u __attribute__((ext_vector_type(4)));
typedef unsigned v2u __attribute__((ext_vector_type(2)));
typedef float f32x4 __attribute__((ext_vector_type(4)));
typedef __bf16 bf16x8 __attribute__((ext_vector_type(8)));
typedef unsigned short u16;

constexpr int DM = 1024, NQ = 4096, SK = 8192, DH = 128, WIN = 2048, FF = 3584;
constexpr int NWG = 256, NTHR = 1024;

constexpr int WS_EPOCH = 0;
constexpr int WS_TS = 1024;
constexpr int WS_QP = 4096;
constexpr int WS_OP = WS_QP + 262144;
constexpr int WS_LS = WS_OP + 1048576;
constexpr int WS_GU = 2 << 20;
constexpr int WS_OUT = WS_GU + 458752;
constexpr int WS_END = WS_OUT + 1048576;

constexpr int L_FLAG = 0;
constexpr int L_TAG = 192;
constexpr int L_TREF = 200;
constexpr int L_XN = 256;
constexpr int L_RED = L_XN + 8192;
constexpr int L_KST = L_RED + 12288;
constexpr int L_QB = L_KST + 32768;
constexpr int L_AO = L_QB + 2304;
constexpr int L_ML = L_AO + 4 * 8 * 528;
constexpr int L_OB = L_ML + 256;
constexpr int L_HB = L_OB + 512;
constexpr int L_END = 84 * 1024;
static_assert(L_HB + 512 <= L_END, "lds map");
enum { F_XN = 0, F_Q, F_GU, F_QB, F_ML, F_AT, F_O, F_H, F_OPUB, F_WPUB };

constexpr int AUX_NT = 2, AUX_SC1 = 16;
constexpr unsigned GSPIN = 1u << 14, LSPIN = 1u << 18;

__device__ __forceinline__ v4u ldnt(const GAS void* p) { return __builtin_nontemporal_load((const GAS v4u*)p); }
__device__ __forceinline__ float asf(unsigned v) { return __uint_as_float(v); }
__device__ __forceinline__ float bflo(unsigned v) { return __uint_as_float(v << 16); }
__device__ __forceinline__ float bfhi(unsigned v) { return __uint_as_float(v & 0xffff0000u); }
__device__ __forceinline__ unsigned pk2(float lo, float hi) {
  typedef __bf16 bf2 __attribute__((ext_vector_type(2)));
  bf2 r; r.x = (__bf16)lo; r.y = (__bf16)hi;
  return __builtin_bit_cast(unsigned, r);
}
__device__ __forceinline__ float rbf(float f) { return (float)(__bf16)f; }
template <int CTRL> __device__ __forceinline__ float dpp(float x) {
  return __builtin_bit_cast(float, __builtin_amdgcn_mov_dpp(__builtin_bit_cast(int, x), CTRL, 0xf, 0xf, true));
}
template <int CTRL> __device__ __forceinline__ int dppi(int x) { return __builtin_amdgcn_mov_dpp(x, CTRL, 0xf, 0xf, true); }
constexpr int XOR1 = 0xB1, XOR2 = 0x4E, HMIR = 0x141, ROR8 = 0x128, ROR4 = 0x124;
__device__ __forceinline__ float tr32(float a, float b) {
  auto t = __builtin_amdgcn_permlane32_swap(__float_as_uint(a), __float_as_uint(b), false, false);
  return __uint_as_float(t[0]) + __uint_as_float(t[1]);
}
__device__ __forceinline__ float tr16(float a, float b) {
  auto t = __builtin_amdgcn_permlane16_swap(__float_as_uint(a), __float_as_uint(b), false, false);
  return __uint_as_float(t[0]) + __uint_as_float(t[1]);
}
__device__ __forceinline__ float tr8(float a, float b, bool bit3) { return (bit3 ? b : a) + dpp<ROR8>(bit3 ? a : b); }
__device__ __forceinline__ float xrow_sum(float x) { x = tr16(x, x); return tr32(x, x); }
__device__ __forceinline__ float xrow_max(float x) {
  auto s = __builtin_amdgcn_permlane16_swap(__float_as_uint(x), __float_as_uint(x), false, false);
  x = fmaxf(__uint_as_float(s[0]), __uint_as_float(s[1]));
  auto t = __builtin_amdgcn_permlane32_swap(__float_as_uint(x), __float_as_uint(x), false, false);
  return fmaxf(__uint_as_float(t[0]), __uint_as_float(t[1]));
}
__device__ __forceinline__ float wave_sum(float x) {
  x += dpp<XOR1>(x); x += dpp<XOR2>(x); x += dpp<HMIR>(x); x += dpp<ROR8>(x);
  return xrow_sum(x);
}
__device__ __forceinline__ f32x4 mfma(v4u a, v4u b, f32x4 c) {
  return __builtin_amdgcn_mfma_f32_16x16x32_bf16(__builtin_bit_cast(bf16x8, a), __builtin_bit_cast(bf16x8, b), c, 0, 0, 0);
}
__device__ __forceinline__ void tpair(const v4u& r0, const v4u& r1, v4u (&F)[8], int m) {
#pragma unroll
  for (int d = 0; d < 4; d++) {
    F[2 * d][m] = __builtin_amdgcn_perm(r1[d], r0[d], 0x05040100u);
    F[2 * d + 1][m] = __builtin_amdgcn_perm(r1[d], r0[d], 0x07060302u);
  }
}
__device__ __forceinline__ unsigned tag1(float a, unsigned tb) { return ((__float_as_uint(a) + 0x80u) & 0xffffff00u) | tb; }
__device__ __forceinline__ v4u tagpack(float a, float b, float c, float d, unsigned tb) { return v4u{tag1(a, tb), tag1(b, tb), tag1(c, tb), tag1(d, tb)}; }
__device__ __forceinline__ unsigned tagbad(const v4u& v, unsigned tb) { return ((v[0] ^ tb) | (v[1] ^ tb) | (v[2] ^ tb) | (v[3] ^ tb)) & 0xffu; }
__device__ __forceinline__ float untag(unsigned v) { return __uint_as_float(v & 0xffffff00u); }

#define SB() __builtin_amdgcn_sched_barrier(0)
#define PIN8(F) asm volatile("" : "+v"(F[0]), "+v"(F[1]), "+v"(F[2]), "+v"(F[3]), "+v"(F[4]), "+v"(F[5]), "+v"(F[6]), "+v"(F[7]))
#define OPAQUE(x) asm volatile("" : "+v"(x) :: "memory")

__device__ __forceinline__ void lds_signal(LAS unsigned char* smem, int f, int w, int lane) {
  asm volatile("s_waitcnt lgkmcnt(0)" ::: "memory");
  if (lane == 0) *(volatile LAS unsigned*)(smem + L_FLAG + f * 16 + w * 4) = 1u;
}
template <int SLEEP = 0>
__device__ __forceinline__ void lds_wait4(LAS unsigned char* smem, int f) {
#pragma unroll 1
  for (unsigned i = 0; i < LSPIN; i++) {
    const v4u v = *(volatile LAS v4u*)(smem + L_FLAG + f * 16);
    if (__builtin_amdgcn_readfirstlane(v[0] & v[1] & v[2] & v[3])) break;
    if (SLEEP) __builtin_amdgcn_s_sleep(SLEEP);
  }
  asm volatile("" ::: "memory");
}
__device__ __forceinline__ unsigned lds_get(LAS unsigned char* smem, int off) {
  v2u v;
#pragma unroll 1
  for (unsigned i = 0; i < LSPIN; i++) {
    v = *(volatile LAS v2u*)(smem + off);
    if (__builtin_amdgcn_readfirstlane(v[1])) break;
    __builtin_amdgcn_s_sleep(2);
  }
  return __builtin_amdgcn_readfirstlane(v[0]);
}
__device__ __forceinline__ unsigned now32() { return (unsigned)__builtin_amdgcn_s_memrealtime(); }
template <int SLEEP>
__device__ __forceinline__ void wait_until(unsigned t) {
#pragma unroll 1
  for (unsigned i = 0; i < LSPIN; i++) {
    if ((int)(now32() - t) >= 0) break;
    __builtin_amdgcn_s_sleep(SLEEP);
  }
}
__device__ __forceinline__ v4u ld_sc1(__amdgpu_buffer_rsrc_t rs, int off) { return __builtin_amdgcn_raw_buffer_load_b128(rs, off, 0, AUX_SC1); }
__device__ __forceinline__ void st_sc1(__amdgpu_buffer_rsrc_t rs, int off, v4u v) { __builtin_amdgcn_raw_buffer_store_b128(v, rs, off, 0, AUX_SC1); }

__global__ void __launch_bounds__(NTHR, 4)
fused_decode(const u16* __restrict__ x_, const u16* __restrict__ kc_, const u16* __restrict__ vc_, const u16* __restrict__ wq_,
             const u16* __restrict__ wo_, const u16* __restrict__ wg_, const u16* __restrict__ wu_, const u16* __restrict__ wd_,
             u16* __restrict__ out_, unsigned char* ws_) {
  __shared__ __attribute__((aligned(16))) unsigned char smem_[L_END];
  LAS unsigned char* smem = (LAS unsigned char*)smem_;
  const int lane = threadIdx.x & 63;
  const int wave = __builtin_amdgcn_readfirstlane(threadIdx.x >> 6);
  const int wg = blockIdx.x, xc = wg & 7, y = wg >> 3;
  const int c16 = lane & 15, kq = lane >> 4, w = wave & 3;
  const __amdgpu_buffer_rsrc_t rs = __builtin_amdgcn_make_buffer_rsrc((void*)ws_, 0, WS_END, 0x00020000);
  const f32x4 z4 = {0.f, 0.f, 0.f, 0.f};
  const unsigned long long tstart = __builtin_amdgcn_s_memrealtime();
  asm volatile("" :: "s"(x_), "s"(kc_), "s"(vc_), "s"(wq_), "s"(wo_), "s"(wg_), "s"(wu_), "s"(wd_), "s"(out_), "s"(ws_), "s"(tstart));
  const int ks = xc >> 1;
  const int frow = 256 * ks + 64 * w + 8 * (lane >> 3);
  const int qcb = y * 2 + (xc & 1);
  const int cq = y & 3, z = y >> 2;
  const int h2 = xc * 4 + (z >> 1), u2 = z & 1;
  const bool has_wd = z != 7;
  const int v2 = xc * 7 + z;
  const int pair = xc * 2 + (y >> 4), sp = y & 15;

  auto tile1 = [&](const v4u (&W)[8], const v4u xq, int mat) {
    const bool par = lane & 8;
    float v[8];
#pragma unroll
    for (int d = 0; d < 4; d++) {
      v4u lo, hi;
#pragma unroll
      for (int m = 0; m < 4; m++) {
        lo[m] = __builtin_amdgcn_perm(W[2 * m + 1][d], W[2 * m][d], 0x05040100u);
        hi[m] = __builtin_amdgcn_perm(W[2 * m + 1][d], W[2 * m][d], 0x07060302u);
      }
      const f32x4 a = mfma(xq, lo, z4), b = mfma(xq, hi, z4);
      v[2 * d] = par ? a[1] : a[0]; v[2 * d + 1] = par ? b[1] : b[0];
    }
    *(LAS f32x4*)(smem + L_RED + mat * 4096 + w * 1024 + kq * 256 + (lane & 7) * 32 + (lane & 8) * 2) =
        f32x4{tr8(v[0], v[4], par), tr8(v[1], v[5], par), tr8(v[2], v[6], par), tr8(v[3], v[7], par)};
  };
  auto red1 = [&](int mat, unsigned tb) {
    f32x4 a = z4;
#pragma unroll
    for (int ww = 0; ww < 4; ww++) a += *(const LAS f32x4*)(smem + L_RED + mat * 4096 + ww * 1024 + lane * 16);
    return tagpack(a[0], a[1], a[2], a[3], tb);
  };
  const int xqoff = L_XN + (c16 >> 2) * 2048 + (256 * ks + 64 * w + 16 * kq + 8 * (c16 & 1)) * 2;

  if (wave < 4) {
    const int b = pair >> 2, g = pair & 3;
    const size_t kv0 = ((size_t)pair * SK + (SK - WIN) + 128 * sp + 32 * w) * (DH * 2);
    __builtin_amdgcn_s_setprio(3);
    const GAS unsigned char* xp = (const GAS unsigned char*)x_ + w * 2048 + lane * 16;
    const v4u x0 = *(const GAS v4u*)xp, x1 = *(const GAS v4u*)(xp + 1024);
    const unsigned epoch = __builtin_amdgcn_raw_buffer_load_b32(rs, WS_EPOCH + (w ? 256 : 0), 0, AUX_SC1);
    SB();
    __builtin_amdgcn_s_barrier();
    SB();
    {
      float v[16];
#pragma unroll
      for (int i = 0; i < 4; i++) { v[2 * i] = bflo(x0[i]); v[2 * i + 1] = bfhi(x0[i]); v[8 + 2 * i] = bflo(x1[i]); v[9 + 2 * i] = bfhi(x1[i]); }
      const float piv = asf(__builtin_amdgcn_readfirstlane(__float_as_uint(v[0])));
      float s = 0.f, q = 0.f;
#pragma unroll
      for (int i = 0; i < 16; i++) { const float d = v[i] - piv; s += d; q += d * d; }
      s = wave_sum(s) * (1.0f / DM); q = wave_sum(q) * (1.0f / DM);
      const float var = fmaxf(q - s * s, 0.f);
      const float mb = rbf(piv + s);
      const float den = rbf(__builtin_amdgcn_sqrtf(rbf(rbf(var) + 1e-5f)));
      const float rinv = __builtin_amdgcn_rcpf(den);
#pragma unroll
      for (int i = 0; i < 16; i++) {
        const float d = rbf(v[i] - mb), q0 = d * rinv;
        v[i] = __builtin_fmaf(__builtin_fmaf(-q0, den, d), rinv, q0);
      }
      *(LAS v4u*)(smem + L_XN + w * 2048 + lane * 16) = v4u{pk2(v[0], v[1]), pk2(v[2], v[3]), pk2(v[4], v[5]), pk2(v[6], v[7])};
      *(LAS v4u*)(smem + L_XN + w * 2048 + 1024 + lane * 16) = v4u{pk2(v[8], v[9]), pk2(v[10], v[11]), pk2(v[12], v[13]), pk2(v[14], v[15])};
    }
    lds_signal(smem, F_XN, w, lane);
    if (w == 0 && lane == 0) *(LAS v2u*)(smem + L_TAG) = v2u{epoch + 1u, 1u};
    asm volatile("" :: "v"(epoch));
    __builtin_amdgcn_s_setprio(1);
    v4u KR[8], VR[8];
#pragma unroll
    for (int i = 0; i < 8; i++) KR[i] = ldnt((const GAS unsigned char*)kc_ + kv0 + i * 1024 + lane * 16);
    SB();
#pragma unroll
    for (int i = 0; i < 8; i++) VR[i] = ldnt((const GAS unsigned char*)vc_ + kv0 + (16 * (i >> 2) + 4 * kq + (i & 3)) * 256 + c16 * 16);
    SB();
    __builtin_amdgcn_s_setprio(3);
    LAS unsigned char* kst = smem + L_KST + w * 8192;
#pragma unroll
    for (int i = 0; i < 8; i++) { const int key = 4 * i + kq; *(LAS v4u*)(kst + key * 256 + ((c16 ^ (key & 15)) * 16)) = KR[i]; }
    SB();
    const unsigned tag = lds_get(smem, L_TAG), tb = tag & 0xffu;
    v4u KF[8];
#pragma unroll
    for (int T = 0; T < 2; T++)
#pragma unroll
      for (int t = 0; t < 4; t++) KF[4 * T + t] = *(const LAS v4u*)(kst + (16 * T + c16) * 256 + (((4 * t + kq) ^ c16) * 16));
    PIN8(KF);
    lds_wait4<1>(smem, F_QB);
    f32x4 S0 = z4, S1 = z4;
#pragma unroll
    for (int t = 0; t < 4; t++) {
      const v4u qf = *(const LAS v4u*)(smem + L_QB + (c16 & 7) * 272 + t * 64 + kq * 16);
      S0 = mfma(KF[t], qf, S0);
      S1 = mfma(KF[4 + t], qf, S1);
    }
    constexpr float SC = 0.08838834764831845f, L2E = 1.4426950408889634f;
    float sv[8] = {S0[0], S0[1], S0[2], S0[3], S1[0], S1[1], S1[2], S1[3]};
#pragma unroll
    for (int i = 0; i < 8; i++) sv[i] = rbf(rbf(sv[i]) * SC) * L2E;
    float m = fmaxf(fmaxf(fmaxf(sv[0], sv[1]), fmaxf(sv[2], sv[3])), fmaxf(fmaxf(sv[4], sv[5]), fmaxf(sv[6], sv[7])));
    m = xrow_max(m);
    float l = 0.f;
#pragma unroll
    for (int i = 0; i < 8; i++) { sv[i] = __builtin_amdgcn_exp2f(sv[i] - m); l += sv[i]; }
    l = xrow_sum(l);
    if (lane < 8) *(LAS v2u*)(smem + L_ML + (w * 8 + lane) * 8) = v2u{__float_as_uint(m), __float_as_uint(l)};
    lds_signal(smem, F_ML, w, lane);
    lds_wait4(smem, F_ML);
    v4u pf;
    float lse;
    {
      float mw[4], lw[4];
#pragma unroll
      for (int ww = 0; ww < 4; ww++) { const v2u t = *(const LAS v2u*)(smem + L_ML + (ww * 8 + (c16 & 7)) * 8); mw[ww] = asf(t[0]); lw[ww] = asf(t[1]); }
      const float M = fmaxf(fmaxf(mw[0], mw[1]), fmaxf(mw[2], mw[3]));
      float L = 0.f;
#pragma unroll
      for (int ww = 0; ww < 4; ww++) L += __builtin_amdgcn_exp2f(mw[ww] - M) * lw[ww];
      const float f = __builtin_amdgcn_exp2f(m - M) * __builtin_amdgcn_rcpf(L);
      pf = v4u{pk2(sv[0] * f, sv[1] * f), pk2(sv[2] * f, sv[3] * f), pk2(sv[4] * f, sv[5] * f), pk2(sv[6] * f, sv[7] * f)};
      lse = M + __builtin_amdgcn_logf(L);
    }
    asm volatile("" : "+v"(pf), "+v"(lse));
    v4u VF[8];
#pragma unroll
    for (int mm = 0; mm < 4; mm++) tpair(VR[2 * mm], VR[2 * mm + 1], VF, mm);
    f32x4 O[8];
#pragma unroll
    for (int j = 0; j < 8; j++) O[j] = mfma(VF[j], pf, z4);
    PIN8(O);
    if (c16 < 8) {
#pragma unroll
      for (int j = 0; j < 8; j++) *(LAS f32x4*)(smem + L_AO + (w * 8 + j) * 528 + kq * 128 + c16 * 16) = O[j];
    }
    lds_signal(smem, F_AT, w, lane);
    if (w == 0 && lane < 8)
      __builtin_amdgcn_raw_buffer_store_b64(v2u{__float_as_uint(lse), tag}, rs, WS_LS + (((g * 8 + lane) * 4 + b) * 16 + sp) * 8, 0, AUX_SC1);
    lds_wait4(smem, F_AT);
    {
      const int hh = 2 * w + (lane >> 5), q4 = (lane >> 3) & 3, j = lane & 7;
      f32x4 a = z4;
#pragma unroll
      for (int ww = 0; ww < 4; ww++) a += *(const LAS f32x4*)(smem + L_AO + (ww * 8 + j) * 528 + q4 * 128 + hh * 16);
      st_sc1(rs, WS_OP + (((g * 8 + hh) * 4 + q4) * 4 + b) * 2048 + sp * 128 + j * 16, tagpack(a[0], a[1], a[2], a[3], tb));
    }
    lds_signal(smem, F_OPUB, w, lane);
  } else if (wave < 8) {
    __builtin_amdgcn_s_setprio(3);
    v4u R[8];
    {
      const __amdgpu_buffer_rsrc_t rq = __builtin_amdgcn_make_buffer_rsrc((void*)wq_, 0, DM * NQ * 2, 0x00020000);
      const int qo = frow * (NQ * 2) + qcb * 128 + (lane & 7) * 16;
#pragma unroll
      for (int i = 0; i < 8; i++) R[i] = __builtin_amdgcn_raw_buffer_load_b128(rq, qo, i * (NQ * 2), AUX_NT);
    }
    SB();
    __builtin_amdgcn_s_barrier();
    SB();
    const int r128 = (wg >> 4) * 8 + (wg & 7), rhf = (wg >> 3) & 1;
    const int oofs = (r128 >> 5) * 2048 + ((r128 & 31) * 32 + rhf * 16 + (lane & 3) * 4 + kq) * 2;
    const unsigned xraw = *(const GAS u16*)((const GAS unsigned char*)x_ + oofs);
    {
      lds_wait4<1>(smem, F_XN);
      const v4u xq = *(const LAS v4u*)(smem + xqoff);
      tile1(R, xq, 0);
    }
    lds_signal(smem, F_Q, w, lane);
    const unsigned tag = lds_get(smem, L_TAG), tb = tag & 0xffu;
    if (w == 3) {
      lds_wait4(smem, F_Q);
      st_sc1(rs, WS_QP + ((kq * 4 + (qcb >> 4)) * 4 + ks) * 4096 + (qcb & 15) * 256 + c16 * 16, red1(0, tb));
    }
    const unsigned tref = lds_get(smem, L_TREF);
    {
      v4u Q[4];
      int qoff = WS_QP + pair * 16384 + w * 1024 + lane * 16;
      wait_until<2>(tref + QGATE);
      unsigned bad = 1;
#pragma unroll 1
      for (unsigned it = 0; it < GSPIN; it++) {
        OPAQUE(qoff);
        if (bad) {
#pragma unroll
          for (int i = 0; i < 4; i++) Q[i] = ld_sc1(rs, qoff + i * 4096);
          bad = tagbad(Q[0], tb) | tagbad(Q[1], tb) | tagbad(Q[2], tb) | tagbad(Q[3], tb);
        }
        if (__all(bad == 0)) break;
      }
      float a[4];
#pragma unroll
      for (int r = 0; r < 4; r++) a[r] = ((untag(Q[0][r]) + untag(Q[1][r])) + untag(Q[2][r])) + untag(Q[3][r]);
      *(LAS v2u*)(smem + L_QB + (2 * w + (lane >> 5)) * 272 + (lane & 31) * 8) = v2u{pk2(a[0], a[1]), pk2(a[2], a[3])};
      lds_signal(smem, F_QB, w, lane);
    }
    {
      const int sl = lane >> 3, j = lane & 7;
      int off = WS_OP + ((h2 * 4 + 2 * u2) * 4 + w) * 2048 + sl * 256 + j * 16;
      const int loff = WS_LS + ((h2 * 4 + w) * 16 + 2 * sl) * 8;
      int oba = L_OB + w * 128 + (lane >> 5) * 64 + j * 8 + ((lane >> 4) & 1) * 4 + ((lane >> 3) & 1) * 2;
      OPAQUE(oba);
      v4u o[4], ls;
      lds_wait4<1>(smem, F_OPUB);
      wait_until<1>(now32() + ODELAY);
      wait_until<1>(tref + OGATE);
      ls = ld_sc1(rs, loff);
      o[0] = ld_sc1(rs, off); o[1] = ld_sc1(rs, off + 128); o[2] = ld_sc1(rs, off + 8192); o[3] = ld_sc1(rs, off + 8320);
      unsigned b0 = tagbad(o[0], tb) | tagbad(o[2], tb) | (ls[1] ^ tag), b1 = tagbad(o[1], tb) | tagbad(o[3], tb) | (ls[3] ^ tag);
      if (!__all((b0 | b1) == 0)) {
#pragma unroll 1
        for (unsigned it = 0; it < GSPIN; it++) {
          __builtin_amdgcn_s_sleep(1);
          OPAQUE(off);
          if (b0 | b1) ls = ld_sc1(rs, loff);
          if (b0) { o[0] = ld_sc1(rs, off); o[2] = ld_sc1(rs, off + 8192); }
          if (b1) { o[1] = ld_sc1(rs, off + 128); o[3] = ld_sc1(rs, off + 8320); }
          b0 = tagbad(o[0], tb) | tagbad(o[2], tb) | (ls[1] ^ tag); b1 = tagbad(o[1], tb) | tagbad(o[3], tb) | (ls[3] ^ tag);
          if (__all((b0 | b1) == 0)) break;
        }
      }
      const float l0 = asf(ls[0]), l1 = asf(ls[2]);
      float M = fmaxf(l0, l1); M = fmaxf(M, dpp<ROR8>(M)); M = xrow_max(M);
      const float e0 = __builtin_amdgcn_exp2f(l0 - M), e1 = __builtin_amdgcn_exp2f(l1 - M);
      float den = e0 + e1; den += dpp<ROR8>(den); den = xrow_sum(den);
      float a[8];
#pragma unroll
      for (int r = 0; r < 4; r++) { a[r] = e0 * untag(o[0][r]) + e1 * untag(o[1][r]); a[4 + r] = e0 * untag(o[2][r]) + e1 * untag(o[3][r]); }
      const float e = tr8(tr16(tr32(a[0], a[4]), tr32(a[2], a[6])), tr16(tr32(a[1], a[5]), tr32(a[3], a[7])), lane & 8) * __builtin_amdgcn_rcpf(den);
      *(LAS u16*)(smem + oba) = (u16)pk2(e, e);
      lds_signal(smem, F_O, w, lane);
    }
    if (w == 0) {
      int off = WS_OUT + r128 * 8192 + (lane >> 2) * 128 + rhf * 64 + (lane & 3) * 16;
      v4u p[4];
      lds_wait4(smem, F_WPUB);
      wait_until<1>(now32() + FDELAY);
      unsigned bad = 1;
#pragma unroll 1
      for (unsigned it = 0; it < GSPIN; it++) {
        OPAQUE(off);
        if (bad) {
#pragma unroll
          for (int i = 0; i < 4; i++) p[i] = ld_sc1(rs, off + i * 2048);
          bad = tagbad(p[0], tb) | tagbad(p[1], tb) | tagbad(p[2], tb) | tagbad(p[3], tb);
        }
        if (__all(bad == 0)) break;
        __builtin_amdgcn_s_sleep(1);
      }
      float a[4];
#pragma unroll
      for (int r = 0; r < 4; r++) a[r] = (untag(p[0][r]) + untag(p[1][r])) + (untag(p[2][r]) + untag(p[3][r]));
      float d = tr16(tr32(a[0], a[2]), tr32(a[1], a[3]));
      d += dpp<ROR8>(d); d += dpp<ROR4>(d);
      d += bflo(xraw);
      if ((lane & 12) == 0) {
        *(GAS u16*)((GAS unsigned char*)out_ + oofs) = (u16)pk2(d, d);
        if (wg == 0 && lane == 0) *(GAS unsigned*)((GAS unsigned char*)ws_ + WS_EPOCH) = tag;
      }
    }
  } else if (wave < 12) {
    __builtin_amdgcn_s_barrier();
    const bool has_g = y >= 4, has_u = (y < 4) || (y >= 8);
    const int yu = (y < 4 ? y : y - 4) + 2;
    const int cbg = (y >= 31 ? y - 31 : y - 3) * 2 + (xc & 1), cbu = (yu >= 28 ? yu - 28 : yu) * 2 + (xc & 1);
    unsigned tb;
    {
      const __amdgpu_buffer_rsrc_t rg = __builtin_amdgcn_make_buffer_rsrc((void*)wg_, 0, has_g ? DM * FF * 2 : 0, 0x00020000);
      const __amdgpu_buffer_rsrc_t ru = __builtin_amdgcn_make_buffer_rsrc((void*)wu_, 0, has_u ? DM * FF * 2 : 0, 0x00020000);
      const int fo = frow * (FF * 2) + (lane & 7) * 16;
      v4u G[8], U[8];
      __builtin_amdgcn_s_setprio(2);
#pragma unroll
      for (int i = 0; i < 8; i++) G[i] = __builtin_amdgcn_raw_buffer_load_b128(rg, fo + (has_g ? cbg : 0) * 128, i * (FF * 2), AUX_NT);
      SB();
#pragma unroll
      for (int i = 0; i < 8; i++) U[i] = __builtin_amdgcn_raw_buffer_load_b128(ru, fo + (has_u ? cbu : 0) * 128, i * (FF * 2), AUX_NT);
      SB();
      __builtin_amdgcn_s_setprio(3);
      lds_wait4<1>(smem, F_XN);
      const v4u xq = *(const LAS v4u*)(smem + xqoff);
      tb = lds_get(smem, L_TAG) & 0xffu;
      tile1(G, xq, 1);
      SB();
      tile1(U, xq, 2);
      lds_signal(smem, F_GU, w, lane);
      if (w < 2 && (w ? has_u : has_g)) {
        lds_wait4(smem, F_GU);
        st_sc1(rs, WS_GU + (w ? cbu : cbg) * 8192 + kq * 2048 + w * 1024 + ks * 256 + c16 * 16, red1(1 + w, tb));
      }
    }
    if (has_wd && w >= 2) {
      v4u g[2], u[2];
      int off = WS_GU + v2 * 8192 + (w - 2) * 4096 + lane * 16;
      wait_until<2>(lds_get(smem, L_TREF) + HGATE);
      unsigned bad = 1;
#pragma unroll 1
      for (unsigned it = 0; it < GSPIN; it++) {
        OPAQUE(off);
        if (bad) {
          g[0] = ld_sc1(rs, off); u[0] = ld_sc1(rs, off + 1024); g[1] = ld_sc1(rs, off + 2048); u[1] = ld_sc1(rs, off + 3072);
          bad = tagbad(g[0], tb) | tagbad(u[0], tb) | tagbad(g[1], tb) | tagbad(u[1], tb);
        }
        if (__all(bad == 0)) break;
        __builtin_amdgcn_s_sleep(4);
      }
#pragma unroll
      for (int t = 0; t < 2; t++) {
        float hv[4];
#pragma unroll
        for (int r = 0; r < 4; r++) hv[r] = rbf(xrow_sum(untag(g[t][r]))) * rbf(xrow_sum(untag(u[t][r])));
        if (lane < 16) *(LAS v2u*)(smem + L_HB + (2 * (w - 2) + t) * 128 + lane * 8) = v2u{pk2(hv[0], hv[1]), pk2(hv[2], hv[3])};
      }
      lds_signal(smem, F_H, w, lane);
      lds_signal(smem, F_H, w - 2, lane);
    }
  } else {
    if (w == 0) {
      if (y == 31 && lane == 0) __builtin_amdgcn_raw_buffer_store_b64(v2u{(unsigned)tstart, (unsigned)(tstart >> 32)}, rs, WS_TS + xc * 128, 0, AUX_SC1);
      if (lane < 52) *(LAS unsigned*)(smem + L_FLAG + lane * 4) = 0u;
    }
    asm volatile("s_waitcnt lgkmcnt(0)" ::: "memory");
    __builtin_amdgcn_s_barrier();
    if (w > 0) {
      __builtin_amdgcn_s_setprio(3);
      wait_until<2>((unsigned)tstart + TSTEP * w);
      int toff = WS_TS + (lane & 7) * 128;
#pragma unroll 1
      for (unsigned it = 0; it < 64; it++) {
        OPAQUE(toff);
        const v2u tv = __builtin_amdgcn_raw_buffer_load_b64(rs, toff, 0, AUX_SC1);
        const unsigned long long d = (((unsigned long long)tv[1] << 32) | tv[0]) - (tstart - 600);
        const bool ok = d < 4000;
        int m = ok ? (int)d : 0;
        m = max(m, dppi<XOR1>(m)); m = max(m, dppi<XOR2>(m)); m = max(m, dppi<HMIR>(m));
        const unsigned done = *(volatile LAS unsigned*)(smem + L_TREF + 4);
        if (__builtin_amdgcn_readfirstlane(done)) break;
        if (__all(ok) || it == 63) {
          if (lane == 0) *(volatile LAS v2u*)(smem + L_TREF) = v2u{(unsigned)tstart - 600u + (unsigned)max(m, 600), 1u};
          break;
        }
      }
      __builtin_amdgcn_s_setprio(0);
    }
    const __amdgpu_buffer_rsrc_t rd = __builtin_amdgcn_make_buffer_rsrc((void*)wd_, 0, has_wd ? FF * DM * 2 : 0, 0x00020000);
    const __amdgpu_buffer_rsrc_t ro = __builtin_amdgcn_make_buffer_rsrc((void*)wo_, 0, NQ * DM * 2, 0x00020000);
    const int r8 = lane >> 3;
    const int cofs = cq * 512 + w * 128 + (lane & 7) * 16;
    const int offd = ((has_wd ? 64 * v2 : 0) + 8 * r8) * (DM * 2) + cofs;
    const int offo = (128 * h2 + 64 * u2 + 32 * (r8 >> 2) + 2 * (r8 & 3)) * (DM * 2) + cofs;
    const int aoff = (c16 >> 2) * 128 + kq * 32 + (c16 & 1) * 16;
    int soff = WS_OUT + (kq * 32 + cq * 8 + w * 2 + ((lane & 7) >> 2)) * 8192 + (h2 * 2 + u2) * 128 + (lane & 3) * 32 + (lane & 8) * 2;
    OPAQUE(soff);
    f32x4 acc[8];
    v4u W[16], F[8];
    wait_until<2>(lds_get(smem, L_TREF) + W2GATE);
    SB();
#pragma unroll
    for (int i = 0; i < 8; i++) W[i] = __builtin_amdgcn_raw_buffer_load_b128(rd, offd, i * (DM * 2), AUX_NT);
#pragma unroll
    for (int i = 0; i < 8; i++) W[8 + i] = __builtin_amdgcn_raw_buffer_load_b128(ro, offo, (8 * (i & 3) + (i >> 2)) * (DM * 2), AUX_NT);
    SB();
    const unsigned tb = lds_get(smem, L_TAG) & 0xffu;
#pragma unroll
    for (int m = 0; m < 4; m++) tpair(W[2 * m], W[2 * m + 1], F, m);
    PIN8(F);
    if (has_wd) lds_wait4<1>(smem, F_H);
    v4u af = *(const LAS v4u*)(smem + L_HB + aoff);
    if (!has_wd) af = v4u{0u, 0u, 0u, 0u};
#pragma unroll
    for (int j = 0; j < 8; j++) acc[j] = mfma(af, F[j], z4);
    PIN8(acc);
#pragma unroll
    for (int m = 0; m < 4; m++) tpair(W[8 + 2 * m], W[9 + 2 * m], F, m);
    PIN8(F);
    lds_wait4(smem, F_O);
    __builtin_amdgcn_s_setprio(3);
    af = *(const LAS v4u*)(smem + L_OB + aoff);
#pragma unroll
    for (int j = 0; j < 8; j++) acc[j] = mfma(af, F[j], acc[j]);
    {
      const bool par = lane & 8;
      float d[4];
#pragma unroll
      for (int j = 0; j < 4; j++) d[j] = (par ? acc[j + 4][1] : acc[j][0]) + dpp<ROR8>(par ? acc[j][1] : acc[j + 4][0]);
      st_sc1(rs, soff, tagpack(d[0], d[1], d[2], d[3], tb));
    }
    lds_signal(smem, F_WPUB, w, lane);
  }
}

extern "C" void kernel_launch(void* const* d_in, const int* in_sizes, int n_in, void* d_out, int out_size, void* d_ws, size_t ws_size,
                              hipStream_t stream) {
  fused_decode<<<NWG, NTHR, 0, stream>>>((const u16*)d_in[0], (const u16*)d_in[1], (const u16*)d_in[2], (const u16*)d_in[3], (const u16*)d_in[4],
                                         (const u16*)d_in[6], (const u16*)d_in[7], (const u16*)d_in[8], (u16*)d_out, (unsigned char*)d_ws);
}
```

```cpp
#include <hip/hip_runtime.h>
#include <stdint.h>

#define QGATE 500
#define W2GATE 650
#define HGATE 710
#define OGATE 990
#define LSLEAD 20
#define ODELAY 40
#define FDELAY 20
#define TSTEP 90

#define GAS __attribute__((address_space(1)))
#define LAS __attribute__((address_space(3)))
typedef unsigned v4u __attribute__((ext_vector_type(4)));
typedef unsigned v2u __attribute__((ext_vector_type(2)));
typedef float f32x4 __attribute__((ext_vector_type(4)));
typedef __bf16 bf16x8 __attribute__((ext_vector_type(8)));
typedef unsigned short u16;

constexpr int DM = 1024, NQ = 4096, SK = 8192, DH = 128, WIN = 2048, FF = 3584;
constexpr int NWG = 256, NTHR = 1024;

constexpr int WS_EPOCH = 0;
constexpr int WS_TS = 1024;
constexpr int WS_QP = 4096;
constexpr int WS_OP = WS_QP + 262144;
constexpr int WS_LS = WS_OP + 1048576;
constexpr int WS_GU = 2 << 20;
constexpr int WS_OUT = WS_GU + 458752;
constexpr int WS_END = WS_OUT + 1048576;

constexpr int L_FLAG = 0;
constexpr int L_TAG = 192;
constexpr int L_TREF = 200;
constexpr int L_XN = 256;
constexpr int L_RED = L_XN + 8192;
constexpr int L_KST = L_RED + 12288;
constexpr int L_QB = L_KST + 32768;
constexpr int L_AO = L_QB + 2304;
constexpr int L_ML = L_AO + 4 * 8 * 528;
constexpr int L_OB = L_ML + 256;
constexpr int L_HB = L_OB + 512;
constexpr int L_END = 84 * 1024;
static_assert(L_HB + 512 <= L_END, "lds map");
enum { F_XN = 0, F_Q, F_GU, F_QB, F_ML, F_AT, F_O, F_H, F_OPUB, F_WPUB };

constexpr int AUX_NT = 2, AUX_SC1 = 16;
constexpr unsigned GSPIN = 1u << 17, LSPIN = 1u << 21;

__device__ __forceinline__ v4u ldnt(const GAS void* p) { return __builtin_nontemporal_load((const GAS v4u*)p); }
__device__ __forceinline__ float asf(unsigned v) { return __uint_as_float(v); }
__device__ __forceinline__ float bflo(unsigned v) { return __uint_as_float(v << 16); }
__device__ __forceinline__ float bfhi(unsigned v) { return __uint_as_float(v & 0xffff0000u); }
__device__ __forceinline__ unsigned pk2(float lo, float hi) {
  typedef __bf16 bf2 __attribute__((ext_vector_type(2)));
  bf2 r; r.x = (__bf16)lo; r.y = (__bf16)hi;
  return __builtin_bit_cast(unsigned, r);
}
__device__ __forceinline__ float rbf(float f) { return (float)(__bf16)f; }
template <int CTRL> __device__ __forceinline__ float dpp(float x) {
  return __builtin_bit_cast(float, __builtin_amdgcn_mov_dpp(__builtin_bit_cast(int, x), CTRL, 0xf, 0xf, true));
}
template <int CTRL> __device__ __forceinline__ int dppi(int x) { return __builtin_amdgcn_mov_dpp(x, CTRL, 0xf, 0xf, true); }
constexpr int XOR1 = 0xB1, XOR2 = 0x4E, HMIR = 0x141, ROR8 = 0x128, ROR4 = 0x124;
__device__ __forceinline__ float tr32(float a, float b) {
  auto t = __builtin_amdgcn_permlane32_swap(__float_as_uint(a), __float_as_uint(b), false, false);
  return __uint_as_float(t[0]) + __uint_as_float(t[1]);
}
__device__ __forceinline__ float tr16(float a, float b) {
  auto t = __builtin_amdgcn_permlane16_swap(__float_as_uint(a), __float_as_uint(b), false, false);
  return __uint_as_float(t[0]) + __uint_as_float(t[1]);
}
__device__ __forceinline__ float tr8(float a, float b, bool bit3) { return (bit3 ? b : a) + dpp<ROR8>(bit3 ? a : b); }
__device__ __forceinline__ float xrow_sum(float x) { x = tr16(x, x); return tr32(x, x); }
__device__ __forceinline__ float xrow_max(float x) {
  auto s = __builtin_amdgcn_permlane16_swap(__float_as_uint(x), __float_as_uint(x), false, false);
  x = fmaxf(__uint_as_float(s[0]), __uint_as_float(s[1]));
  auto t = __builtin_amdgcn_permlane32_swap(__float_as_uint(x), __float_as_uint(x), false, false);
  return fmaxf(__uint_as_float(t[0]), __uint_as_float(t[1]));
}
__device__ __forceinline__ float wave_sum(float x) {
  x += dpp<XOR1>(x); x += dpp<XOR2>(x); x += dpp<HMIR>(x); x += dpp<ROR8>(x);
  return xrow_sum(x);
}
__device__ __forceinline__ f32x4 mfma(v4u a, v4u b, f32x4 c) {
  return __builtin_amdgcn_mfma_f32_16x16x32_bf16(__builtin_bit_cast(bf16x8, a), __builtin_bit_cast(bf16x8, b), c, 0, 0, 0);
}
__device__ __forceinline__ void tpair(const v4u& r0, const v4u& r1, v4u (&F)[8], int m) {
#pragma unroll
  for (int d = 0; d < 4; d++) {
    F[2 * d][m] = __builtin_amdgcn_perm(r1[d], r0[d], 0x05040100u);
    F[2 * d + 1][m] = __builtin_amdgcn_perm(r1[d], r0[d], 0x07060302u);
  }
}
struct Tag4 { unsigned b0, b1, b2, b3; };
__device__ __forceinline__ Tag4 tag4(unsigned t) { return Tag4{t & 0xffu, (t + (t >> 8)) & 0xffu, (t + (t >> 16)) & 0xffu, (t + (t >> 24)) & 0xffu}; }
__device__ __forceinline__ unsigned tag1(float a, unsigned tb) { return ((__float_as_uint(a) + 0x80u) & 0xffffff00u) | tb; }
__device__ __forceinline__ v4u tagpack(float a, float b, float c, float d, const Tag4 tb) { return v4u{tag1(a, tb.b0), tag1(b, tb.b1), tag1(c, tb.b2), tag1(d, tb.b3)}; }
__device__ __forceinline__ unsigned tagbad(const v4u& v, const Tag4 tb) { return ((v[0] ^ tb.b0) | (v[1] ^ tb.b1) | (v[2] ^ tb.b2) | (v[3] ^ tb.b3)) & 0xffu; }
__device__ __forceinline__ float untag(unsigned v) { return __uint_as_float(v & 0xffffff00u); }

#define SB() __builtin_amdgcn_sched_barrier(0)
#define PIN8(F) asm volatile("" : "+v"(F[0]), "+v"(F[1]), "+v"(F[2]), "+v"(F[3]), "+v"(F[4]), "+v"(F[5]), "+v"(F[6]), "+v"(F[7]))
#define OPAQUE(x) asm volatile("" : "+v"(x) :: "memory")

__device__ __forceinline__ void lds_signal(LAS unsigned char* smem, int f, int w, int lane) {
  asm volatile("s_waitcnt lgkmcnt(0)" ::: "memory");
  if (lane == 0) *(volatile LAS unsigned*)(smem + L_FLAG + f * 16 + w * 4) = 1u;
}
template <int SLEEP = 0>
__device__ __forceinline__ void lds_wait4(LAS unsigned char* smem, int f) {
#pragma unroll 1
  for (unsigned i = 0; i < LSPIN; i++) {
    const v4u v = *(volatile LAS v4u*)(smem + L_FLAG + f * 16);
    if (__builtin_amdgcn_readfirstlane(v[0] & v[1] & v[2] & v[3])) break;
    if (SLEEP) __builtin_amdgcn_s_sleep(SLEEP);
  }
  asm volatile("" ::: "memory");
}
__device__ __forceinline__ unsigned lds_get(LAS unsigned char* smem, int off) {
  v2u v;
#pragma unroll 1
  for (unsigned i = 0; i < LSPIN; i++) {
    v = *(volatile LAS v2u*)(smem + off);
    if (__builtin_amdgcn_readfirstlane(v[1])) break;
    __builtin_amdgcn_s_sleep(2);
  }
  return __builtin_amdgcn_readfirstlane(v[0]);
}
__device__ __forceinline__ unsigned now32() { return (unsigned)__builtin_amdgcn_s_memrealtime(); }
template <int SLEEP>
__device__ __forceinline__ void wait_until(unsigned t) {
#pragma unroll 1
  for (unsigned i = 0; i < LSPIN; i++) {
    if ((int)(now32() - t) >= 0) break;
    __builtin_amdgcn_s_sleep(SLEEP);
  }
}
__device__ __forceinline__ v4u ld_sc1(__amdgpu_buffer_rsrc_t rs, int off) { return __builtin_amdgcn_raw_buffer_load_b128(rs, off, 0, AUX_SC1); }
__device__ __forceinline__ void st_sc1(__amdgpu_buffer_rsrc_t rs, int off, v4u v) { __builtin_amdgcn_raw_buffer_store_b128(v, rs, off, 0, AUX_SC1); }

__global__ void __launch_bounds__(NTHR, 4)
fused_decode(const u16* __restrict__ x_, const u16* __restrict__ kc_, const u16* __restrict__ vc_, const u16* __restrict__ wq_,
             const u16* __restrict__ wo_, const u16* __restrict__ wg_, const u16* __restrict__ wu_, const u16* __restrict__ wd_,
             u16* __restrict__ out_, unsigned char* ws_) {
  __shared__ __attribute__((aligned(16))) unsigned char smem_[L_END];
  LAS unsigned char* smem = (LAS unsigned char*)smem_;
  const int lane = threadIdx.x & 63;
  const int wave = __builtin_amdgcn_readfirstlane(threadIdx.x >> 6);
  const int wg = blockIdx.x, xc = wg & 7, y = wg >> 3;
  const int c16 = lane & 15, kq = lane >> 4, w = wave & 3;
  const __amdgpu_buffer_rsrc_t rs = __builtin_amdgcn_make_buffer_rsrc((void*)ws_, 0, WS_END, 0x00020000);
  const f32x4 z4 = {0.f, 0.f, 0.f, 0.f};
  const unsigned long long tstart = __builtin_amdgcn_s_memrealtime();
  asm volatile("" :: "s"(x_), "s"(kc_), "s"(vc_), "s"(wq_), "s"(wo_), "s"(wg_), "s"(wu_), "s"(wd_), "s"(out_), "s"(ws_), "s"(tstart));
  const int ks = xc >> 1;
  const int frow = 256 * ks + 64 * w + 8 * (lane >> 3);
  const int qcb = y * 2 + (xc & 1);
  const int cq = y & 3, z = y >> 2;
  const int h2 = xc * 4 + (z >> 1), u2 = z & 1;
  const bool has_wd = z != 7;
  const int v2 = xc * 7 + z;
  const int pair = xc * 2 + (y >> 4), sp = y & 15;

  auto tile1 = [&](const v4u (&W)[8], const v4u xq, int mat) {
    const bool par = lane & 8;
    float v[8];
#pragma unroll
    for (int d = 0; d < 4; d++) {
      v4u lo, hi;
#pragma unroll
      for (int m = 0; m < 4; m++) {
        lo[m] = __builtin_amdgcn_perm(W[2 * m + 1][d], W[2 * m][d], 0x05040100u);
        hi[m] = __builtin_amdgcn_perm(W[2 * m + 1][d], W[2 * m][d], 0x07060302u);
      }
      const f32x4 a = mfma(xq, lo, z4), b = mfma(xq, hi, z4);
      v[2 * d] = par ? a[1] : a[0]; v[2 * d + 1] = par ? b[1] : b[0];
    }
    *(LAS f32x4*)(smem + L_RED + mat * 4096 + w * 1024 + kq * 256 + (lane & 7) * 32 + (lane & 8) * 2) =
        f32x4{tr8(v[0], v[4], par), tr8(v[1], v[5], par), tr8(v[2], v[6], par), tr8(v[3], v[7], par)};
  };
  auto red1 = [&](int mat, const Tag4 tb) {
    f32x4 a = z4;
#pragma unroll
    for (int ww = 0; ww < 4; ww++) a += *(const LAS f32x4*)(smem + L_RED + mat * 4096 + ww * 1024 + lane * 16);
    return tagpack(a[0], a[1], a[2], a[3], tb);
  };
  const int xqoff = L_XN + (c16 >> 2) * 2048 + (256 * ks + 64 * w + 16 * kq + 8 * (c16 & 1)) * 2;

  if (wave < 4) {
    const int b = pair >> 2, g = pair & 3;
    const size_t kv0 = ((size_t)pair * SK + (SK - WIN) + 128 * sp + 32 * w) * (DH * 2);
    __builtin_amdgcn_s_setprio(3);
    const GAS unsigned char* xp = (const GAS unsigned char*)x_ + w * 2048 + lane * 16;
    const v4u x0 = *(const GAS v4u*)xp, x1 = *(const GAS v4u*)(xp + 1024);
    const unsigned epoch = __builtin_amdgcn_raw_buffer_load_b32(rs, WS_EPOCH + (w ? 256 : 0), 0, AUX_SC1);
    SB();
    __builtin_amdgcn_s_barrier();
    SB();
    {
      float v[16];
#pragma unroll
      for (int i = 0; i < 4; i++) { v[2 * i] = bflo(x0[i]); v[2 * i + 1] = bfhi(x0[i]); v[8 + 2 * i] = bflo(x1[i]); v[9 + 2 * i] = bfhi(x1[i]); }
      const float piv = asf(__builtin_amdgcn_readfirstlane(__float_as_uint(v[0])));
      float s = 0.f, q = 0.f;
#pragma unroll
      for (int i = 0; i < 16; i++) { const float d = v[i] - piv; s += d; q += d * d; }
      s = wave_sum(s) * (1.0f / DM); q = wave_sum(q) * (1.0f / DM);
      const float var = fmaxf(q - s * s, 0.f);
      const float mb = rbf(piv + s);
      const float den = rbf(__builtin_amdgcn_sqrtf(rbf(rbf(var) + 1e-5f)));
      const float rinv = __builtin_amdgcn_rcpf(den);
#pragma unroll
      for (int i = 0; i < 16; i++) {
        const float d = rbf(v[i] - mb), q0 = d * rinv;
        v[i] = __builtin_fmaf(__builtin_fmaf(-q0, den, d), rinv, q0);
      }
      *(LAS v4u*)(smem + L_XN + w * 2048 + lane * 16) = v4u{pk2(v[0], v[1]), pk2(v[2], v[3]), pk2(v[4], v[5]), pk2(v[6], v[7])};
      *(LAS v4u*)(smem + L_XN + w * 2048 + 1024 + lane * 16) = v4u{pk2(v[8], v[9]), pk2(v[10], v[11]), pk2(v[12], v[13]), pk2(v[14], v[15])};
    }
    lds_signal(smem, F_XN, w, lane);
    if (w == 0 && lane == 0) *(LAS v2u*)(smem + L_TAG) = v2u{epoch + 1u, 1u};
    asm volatile("" :: "v"(epoch));
    __builtin_amdgcn_s_setprio(1);
    v4u KR[8], VR[8];
#pragma unroll
    for (int i = 0; i < 8; i++) KR[i] = ldnt((const GAS unsigned char*)kc_ + kv0 + i * 1024 + lane * 16);
    SB();
#pragma unroll
    for (int i = 0; i < 8; i++) VR[i] = ldnt((const GAS unsigned char*)vc_ + kv0 + (16 * (i >> 2) + 4 * kq + (i & 3)) * 256 + c16 * 16);
    SB();
    __builtin_amdgcn_s_setprio(3);
    LAS unsigned char* kst = smem + L_KST + w * 8192;
#pragma unroll
    for (int i = 0; i < 8; i++) { const int key = 4 * i + kq; *(LAS v4u*)(kst + key * 256 + ((c16 ^ (key & 15)) * 16)) = KR[i]; }
    SB();
    const unsigned tag = lds_get(smem, L_TAG);
    const Tag4 tb = tag4(tag);
    v4u KF[8];
#pragma unroll
    for (int T = 0; T < 2; T++)
#pragma unroll
      for (int t = 0; t < 4; t++) KF[4 * T + t] = *(const LAS v4u*)(kst + (16 * T + c16) * 256 + (((4 * t + kq) ^ c16) * 16));
    PIN8(KF);
    lds_wait4<1>(smem, F_QB);
    f32x4 S0 = z4, S1 = z4;
#pragma unroll
    for (int t = 0; t < 4; t++) {
      const v4u qf = *(const LAS v4u*)(smem + L_QB + (c16 & 7) * 272 + t * 64 + kq * 16);
      S0 = mfma(KF[t], qf, S0);
      S1 = mfma(KF[4 + t], qf, S1);
    }
    constexpr float SC = 0.08838834764831845f, L2E = 1.4426950408889634f;
    float sv[8] = {S0[0], S0[1], S0[2], S0[3], S1[0], S1[1], S1[2], S1[3]};
#pragma unroll
    for (int i = 0; i < 8; i++) sv[i] = rbf(rbf(sv[i]) * SC) * L2E;
    float m = fmaxf(fmaxf(fmaxf(sv[0], sv[1]), fmaxf(sv[2], sv[3])), fmaxf(fmaxf(sv[4], sv[5]), fmaxf(sv[6], sv[7])));
    m = xrow_max(m);
    float l = 0.f;
#pragma unroll
    for (int i = 0; i < 8; i++) { sv[i] = __builtin_amdgcn_exp2f(sv[i] - m); l += sv[i]; }
    l = xrow_sum(l);
    if (lane < 8) *(LAS v2u*)(smem + L_ML + (w * 8 + lane) * 8) = v2u{__float_as_uint(m), __float_as_uint(l)};
    lds_signal(smem, F_ML, w, lane);
    lds_wait4(smem, F_ML);
    v4u pf;
    float lse;
    {
      float mw[4], lw[4];
#pragma unroll
      for (int ww = 0; ww < 4; ww++) { const v2u t = *(const LAS v2u*)(smem + L_ML + (ww * 8 + (c16 & 7)) * 8); mw[ww] = asf(t[0]); lw[ww] = asf(t[1]); }
      const float M = fmaxf(fmaxf(mw[0], mw[1]), fmaxf(mw[2], mw[3]));
      float L = 0.f;
#pragma unroll
      for (int ww = 0; ww < 4; ww++) L += __builtin_amdgcn_exp2f(mw[ww] - M) * lw[ww];
      const float f = __builtin_amdgcn_exp2f(m - M) * __builtin_amdgcn_rcpf(L);
      pf = v4u{pk2(sv[0] * f, sv[1] * f), pk2(sv[2] * f, sv[3] * f), pk2(sv[4] * f, sv[5] * f), pk2(sv[6] * f, sv[7] * f)};
      lse = M + __builtin_amdgcn_logf(L);
    }
    asm volatile("" : "+v"(pf), "+v"(lse));
    v4u VF[8];
#pragma unroll
    for (int mm = 0; mm < 4; mm++) tpair(VR[2 * mm], VR[2 * mm + 1], VF, mm);
    f32x4 O[8];
#pragma unroll
    for (int j = 0; j < 8; j++) O[j] = mfma(VF[j], pf, z4);
    PIN8(O);
    if (c16 < 8) {
#pragma unroll
      for (int j = 0; j < 8; j++) *(LAS f32x4*)(smem + L_AO + (w * 8 + j) * 528 + kq * 128 + c16 * 16) = O[j];
    }
    lds_signal(smem, F_AT, w, lane);
    if (w == 0 && lane < 8)
      __builtin_amdgcn_raw_buffer_store_b64(v2u{__float_as_uint(lse), tag}, rs, WS_LS + (((g * 8 + lane) * 4 + b) * 16 + sp) * 8, 0, AUX_SC1);
    lds_wait4(smem, F_AT);
    {
      const int hh = 2 * w + (lane >> 5), q4 = (lane >> 3) & 3, j = lane & 7;
      f32x4 a = z4;
#pragma unroll
      for (int ww = 0; ww < 4; ww++) a += *(const LAS f32x4*)(smem + L_AO + (ww * 8 + j) * 528 + q4 * 128 + hh * 16);
      st_sc1(rs, WS_OP + (((g * 8 + hh) * 4 + q4) * 4 + b) * 2048 + sp * 128 + j * 16, tagpack(a[0], a[1], a[2], a[3], tb));
    }
    lds_signal(smem, F_OPUB, w, lane);
  } else if (wave < 8) {
    __builtin_amdgcn_s_setprio(3);
    v4u R[8];
    {
      const __amdgpu_buffer_rsrc_t rq = __builtin_amdgcn_make_buffer_rsrc((void*)wq_, 0, DM * NQ * 2, 0x00020000);
      const int qo = frow * (NQ * 2) + qcb * 128 + (lane & 7) * 16;
#pragma unroll
      for (int i = 0; i < 8; i++) R[i] = __builtin_amdgcn_raw_buffer_load_b128(rq, qo, i * (NQ * 2), AUX_NT);
    }
    SB();
    __builtin_amdgcn_s_barrier();
    SB();
    const int r128 = (wg >> 4) * 8 + (wg & 7), rhf = (wg >> 3) & 1;
    const int oofs = (r128 >> 5) * 2048 + ((r128 & 31) * 32 + rhf * 16 + (lane & 3) * 4 + kq) * 2;
    const unsigned xraw = *(const GAS u16*)((const GAS unsigned char*)x_ + oofs);
    {
      lds_wait4<1>(smem, F_XN);
      const v4u xq = *(const LAS v4u*)(smem + xqoff);
      tile1(R, xq, 0);
    }
    lds_signal(smem, F_Q, w, lane);
    const unsigned tag = lds_get(smem, L_TAG);
    const Tag4 tb = tag4(tag);
    if (w == 3) {
      lds_wait4(smem, F_Q);
      st_sc1(rs, WS_QP + ((kq * 4 + (qcb >> 4)) * 4 + ks) * 4096 + (qcb & 15) * 256 + c16 * 16, red1(0, tb));
    }
    {
      v4u Q[4];
      int qoff = WS_QP + pair * 16384 + w * 1024 + lane * 16;
      wait_until<2>((unsigned)tstart + QGATE);
      const v2u tr = *(volatile LAS v2u*)(smem + L_TREF);
      if (__builtin_amdgcn_readfirstlane(tr[1])) wait_until<2>(__builtin_amdgcn_readfirstlane(tr[0]) + QGATE);
      unsigned bad = 1;
#pragma unroll 1
      for (unsigned it = 0; it < GSPIN; it++) {
        OPAQUE(qoff);
        if (bad) {
#pragma unroll
          for (int i = 0; i < 4; i++) Q[i] = ld_sc1(rs, qoff + i * 4096);
          bad = tagbad(Q[0], tb) | tagbad(Q[1], tb) | tagbad(Q[2], tb) | tagbad(Q[3], tb);
        }
        if (__all(bad == 0)) break;
      }
      float a[4];
#pragma unroll
      for (int r = 0; r < 4; r++) a[r] = ((untag(Q[0][r]) + untag(Q[1][r])) + untag(Q[2][r])) + untag(Q[3][r]);
      *(LAS v2u*)(smem + L_QB + (2 * w + (lane >> 5)) * 272 + (lane & 31) * 8) = v2u{pk2(a[0], a[1]), pk2(a[2], a[3])};
      lds_signal(smem, F_QB, w, lane);
    }
    {
      const int sl = lane >> 3, j = lane & 7;
      int off = WS_OP + ((h2 * 4 + 2 * u2) * 4 + w) * 2048 + sl * 256 + j * 16;
      const int loff = WS_LS + ((h2 * 4 + w) * 16 + 2 * sl) * 8;
      int oba = L_OB + w * 128 + (lane >> 5) * 64 + j * 8 + ((lane >> 4) & 1) * 4 + ((lane >> 3) & 1) * 2;
      OPAQUE(oba);
      v4u o[4], ls;
      const unsigned tref = lds_get(smem, L_TREF);
      lds_wait4<1>(smem, F_OPUB);
      wait_until<1>(now32() + ODELAY);
      wait_until<1>(tref + OGATE - LSLEAD);
      ls = ld_sc1(rs, loff);
      SB();
      wait_until<1>(tref + OGATE);
      o[0] = ld_sc1(rs, off); o[1] = ld_sc1(rs, off + 128); o[2] = ld_sc1(rs, off + 8192); o[3] = ld_sc1(rs, off + 8320);
      SB();
      float e0, e1, den;
      auto weights = [&]() {
        const float l0 = asf(ls[0]), l1 = asf(ls[2]);
        float M = fmaxf(l0, l1); M = fmaxf(M, dpp<ROR8>(M)); M = xrow_max(M);
        e0 = __builtin_amdgcn_exp2f(l0 - M); e1 = __builtin_amdgcn_exp2f(l1 - M);
        den = e0 + e1; den += dpp<ROR8>(den); den = __builtin_amdgcn_rcpf(xrow_sum(den));
      };
      weights();
      asm volatile("" : "+v"(e0), "+v"(e1), "+v"(den));
      SB();
      unsigned b0 = tagbad(o[0], tb) | tagbad(o[2], tb) | (ls[1] ^ tag), b1 = tagbad(o[1], tb) | tagbad(o[3], tb) | (ls[3] ^ tag);
      if (!__all((b0 | b1) == 0)) {
#pragma unroll 1
        for (unsigned it = 0; it < GSPIN; it++) {
          __builtin_amdgcn_s_sleep(1);
          OPAQUE(off);
          if (b0 | b1) ls = ld_sc1(rs, loff);
          if (b0) { o[0] = ld_sc1(rs, off); o[2] = ld_sc1(rs, off + 8192); }
          if (b1) { o[1] = ld_sc1(rs, off + 128); o[3] = ld_sc1(rs, off + 8320); }
          b0 = tagbad(o[0], tb) | tagbad(o[2], tb) | (ls[1] ^ tag); b1 = tagbad(o[1], tb) | tagbad(o[3], tb) | (ls[3] ^ tag);
          if (__all((b0 | b1) == 0)) break;
        }
        weights();
      }
      float a[8];
#pragma unroll
      for (int r = 0; r < 4; r++) { a[r] = e0 * untag(o[0][r]) + e1 * untag(o[1][r]); a[4 + r] = e0 * untag(o[2][r]) + e1 * untag(o[3][r]); }
      const float e = tr8(tr16(tr32(a[0], a[4]), tr32(a[2], a[6])), tr16(tr32(a[1], a[5]), tr32(a[3], a[7])), lane & 8) * den;
      *(LAS u16*)(smem + oba) = (u16)pk2(e, e);
      lds_signal(smem, F_O, w, lane);
    }
    if (w == 0) {
      int off = WS_OUT + r128 * 8192 + (lane >> 2) * 128 + rhf * 64 + (lane & 3) * 16;
      v4u p[4];
      lds_wait4(smem, F_WPUB);
      wait_until<1>(now32() + FDELAY);
      unsigned bad = 1;
#pragma unroll 1
      for (unsigned it = 0; it < GSPIN; it++) {
        OPAQUE(off);
        if (bad) {
#pragma unroll
          for (int i = 0; i < 4; i++) p[i] = ld_sc1(rs, off + i * 2048);
          bad = tagbad(p[0], tb) | tagbad(p[1], tb) | tagbad(p[2], tb) | tagbad(p[3], tb);
        }
        if (__all(bad == 0)) break;
        __builtin_amdgcn_s_sleep(1);
      }
      float a[4];
#pragma unroll
      for (int r = 0; r < 4; r++) a[r] = (untag(p[0][r]) + untag(p[1][r])) + (untag(p[2][r]) + untag(p[3][r]));
      float d = tr16(tr32(a[0], a[2]), tr32(a[1], a[3]));
      d += dpp<ROR8>(d); d += dpp<ROR4>(d);
      d += bflo(xraw);
      if ((lane & 12) == 0) {
        *(GAS u16*)((GAS unsigned char*)out_ + oofs) = (u16)pk2(d, d);
        if (wg == 0 && lane == 0) __builtin_amdgcn_raw_buffer_store_b32(tag, rs, WS_EPOCH, 0, AUX_SC1);
      }
    }
  } else if (wave < 12) {
    __builtin_amdgcn_s_barrier();
    const bool has_g = y >= 4, has_u = (y < 4) || (y >= 8);
    const int yu = (y < 4 ? y : y - 4) + 2;
    const int cbg = (y >= 31 ? y - 31 : y - 3) * 2 + (xc & 1), cbu = (yu >= 28 ? yu - 28 : yu) * 2 + (xc & 1);
    Tag4 tb;
    {
      const __amdgpu_buffer_rsrc_t rg = __builtin_amdgcn_make_buffer_rsrc((void*)wg_, 0, has_g ? DM * FF * 2 : 0, 0x00020000);
      const __amdgpu_buffer_rsrc_t ru = __builtin_amdgcn_make_buffer_rsrc((void*)wu_, 0, has_u ? DM * FF * 2 : 0, 0x00020000);
      const int fo = frow * (FF * 2) + (lane & 7) * 16;
      v4u G[8], U[8];
      __builtin_amdgcn_s_setprio(2);
#pragma unroll
      for (int i = 0; i < 8; i++) G[i] = __builtin_amdgcn_raw_buffer_load_b128(rg, fo + (has_g ? cbg : 0) * 128, i * (FF * 2), AUX_NT);
      SB();
#pragma unroll
      for (int i = 0; i < 8; i++) U[i] = __builtin_amdgcn_raw_buffer_load_b128(ru, fo + (has_u ? cbu : 0) * 128, i * (FF * 2), AUX_NT);
      SB();
      __builtin_amdgcn_s_setprio(3);
      lds_wait4<1>(smem, F_XN);
      const v4u xq = *(const LAS v4u*)(smem + xqoff);
      tb = tag4(lds_get(smem, L_TAG));
      tile1(G, xq, 1);
      SB();
      tile1(U, xq, 2);
      lds_signal(smem, F_GU, w, lane);
      if (w < 2 && (w ? has_u : has_g)) {
        lds_wait4(smem, F_GU);
        st_sc1(rs, WS_GU + (w ? cbu : cbg) * 8192 + kq * 2048 + w * 1024 + ks * 256 + c16 * 16, red1(1 + w, tb));
      }
    }
    if (has_wd && w >= 2) {
      v4u g[2], u[2];
      int off = WS_GU + v2 * 8192 + (w - 2) * 4096 + lane * 16;
      wait_until<2>(lds_get(smem, L_TREF) + HGATE);
      unsigned bad = 1;
#pragma unroll 1
      for (unsigned it = 0; it < GSPIN; it++) {
        OPAQUE(off);
        if (bad) {
          g[0] = ld_sc1(rs, off); u[0] = ld_sc1(rs, off + 1024); g[1] = ld_sc1(rs, off + 2048); u[1] = ld_sc1(rs, off + 3072);
          bad = tagbad(g[0], tb) | tagbad(u[0], tb) | tagbad(g[1], tb) | tagbad(u[1], tb);
        }
        if (__all(bad == 0)) break;
        __builtin_amdgcn_s_sleep(4);
      }
#pragma unroll
      for (int t = 0; t < 2; t++) {
        float hv[4];
#pragma unroll
        for (int r = 0; r < 4; r++) hv[r] = rbf(xrow_sum(untag(g[t][r]))) * rbf(xrow_sum(untag(u[t][r])));
        if (lane < 16) *(LAS v2u*)(smem + L_HB + (2 * (w - 2) + t) * 128 + lane * 8) = v2u{pk2(hv[0], hv[1]), pk2(hv[2], hv[3])};
      }
      lds_signal(smem, F_H, w, lane);
      lds_signal(smem, F_H, w - 2, lane);
    }
  } else {
    if (w == 0) {
      if (y == 31 && lane == 0) __builtin_amdgcn_raw_buffer_store_b64(v2u{(unsigned)tstart, (unsigned)(tstart >> 32)}, rs, WS_TS + xc * 128, 0, AUX_SC1);
      if (lane < 52) *(LAS unsigned*)(smem + L_FLAG + lane * 4) = 0u;
    }
    asm volatile("s_waitcnt lgkmcnt(0)" ::: "memory");
    __builtin_amdgcn_s_barrier();
    if (w > 0) {
      __builtin_amdgcn_s_setprio(3);
      wait_until<2>((unsigned)tstart + TSTEP * w);
      int toff = WS_TS + (lane & 7) * 128;
#pragma unroll 1
      for (unsigned it = 0; it < 64; it++) {
        OPAQUE(toff);
        const v2u tv = __builtin_amdgcn_raw_buffer_load_b64(rs, toff, 0, AUX_SC1);
        const unsigned long long d = (((unsigned long long)tv[1] << 32) | tv[0]) - (tstart - 600);
        const bool ok = d < 4000;
        int m = ok ? (int)d : 0;
        m = max(m, dppi<XOR1>(m)); m = max(m, dppi<XOR2>(m)); m = max(m, dppi<HMIR>(m));
        const unsigned done = *(volatile LAS unsigned*)(smem + L_TREF + 4);
        if (__builtin_amdgcn_readfirstlane(done)) break;
        if (__all(ok) || it == 63) {
          if (lane == 0) *(volatile LAS v2u*)(smem + L_TREF) = v2u{(unsigned)tstart - 600u + (unsigned)max(m, 600), 1u};
          break;
        }
      }
      __builtin_amdgcn_s_setprio(0);
    }
    const __amdgpu_buffer_rsrc_t rd = __builtin_amdgcn_make_buffer_rsrc((void*)wd_, 0, has_wd ? FF * DM * 2 : 0, 0x00020000);
    const __amdgpu_buffer_rsrc_t ro = __builtin_amdgcn_make_buffer_rsrc((void*)wo_, 0, NQ * DM * 2, 0x00020000);
    const int r8 = lane >> 3;
    const int cofs = cq * 512 + w * 128 + (lane & 7) * 16;
    const int offd = ((has_wd ? 64 * v2 : 0) + 8 * r8) * (DM * 2) + cofs;
    const int offo = (128 * h2 + 64 * u2 + 32 * (r8 >> 2) + 2 * (r8 & 3)) * (DM * 2) + cofs;
    const int aoff = (c16 >> 2) * 128 + kq * 32 + (c16 & 1) * 16;
    int soff = WS_OUT + (kq * 32 + cq * 8 + w * 2 + ((lane & 7) >> 2)) * 8192 + (h2 * 2 + u2) * 128 + (lane & 3) * 32 + (lane & 8) * 2;
    OPAQUE(soff);
    f32x4 acc[8];
    v4u W[16], F[8];
    wait_until<2>(lds_get(smem, L_TREF) + W2GATE);
    SB();
#pragma unroll
    for (int i = 0; i < 8; i++) W[i] = __builtin_amdgcn_raw_buffer_load_b128(rd, offd, i * (DM * 2), AUX_NT);
#pragma unroll
    for (int i = 0; i < 8; i++) W[8 + i] = __builtin_amdgcn_raw_buffer_load_b128(ro, offo, (8 * (i & 3) + (i >> 2)) * (DM * 2), AUX_NT);
    SB();
    const Tag4 tb = tag4(lds_get(smem, L_TAG));
#pragma unroll
    for (int m = 0; m < 4; m++) tpair(W[2 * m], W[2 * m + 1], F, m);
    PIN8(F);
    if (has_wd) lds_wait4<1>(smem, F_H);
    v4u af = *(const LAS v4u*)(smem + L_HB + aoff);
    if (!has_wd) af = v4u{0u, 0u, 0u, 0u};
#pragma unroll
    for (int j = 0; j < 8; j++) acc[j] = mfma(af, F[j], z4);
    PIN8(acc);
#pragma unroll
    for (int m = 0; m < 4; m++) tpair(W[8 + 2 * m], W[9 + 2 * m], F, m);
    PIN8(F);
    lds_wait4(smem, F_O);
    __builtin_amdgcn_s_setprio(3);
    af = *(const LAS v4u*)(smem + L_OB + aoff);
#pragma unroll
    for (int j = 0; j < 8; j++) acc[j] = mfma(af, F[j], acc[j]);
    {
      const bool par = lane & 8;
      float d[4];
#pragma unroll
      for (int j = 0; j < 4; j++) d[j] = (par ? acc[j + 4][1] : acc[j][0]) + dpp<ROR8>(par ? acc[j][1] : acc[j + 4][0]);
      st_sc1(rs, soff, tagpack(d[0], d[1], d[2], d[3], tb));
    }
    lds_signal(smem, F_WPUB, w, lane);
  }
}

extern "C" void kernel_launch(void* const* d_in, const int* in_sizes, int n_in, void* d_out, int out_size, void* d_ws, size_t ws_size,
                              hipStream_t stream) {
  fused_decode<<<NWG, NTHR, 0, stream>>>((const u16*)d_in[0], (const u16*)d_in[1], (const u16*)d_in[2], (const u16*)d_in[3], (const u16*)d_in[4],
                                         (const u16*)d_in[6], (const u16*)d_in[7], (const u16*)d_in[8], (u16*)d_out, (unsigned char*)d_ws);
}
```
